```python
import torch

def _softmax(x, axis=-1):
    return torch.softmax(x, dim=axis)
def _gelu(x):
    return torch.nn.functional.gelu(x, approximate="tanh")
def _silu(x):
    return torch.nn.functional.silu(x)
def _sigmoid(x):
    return torch.sigmoid(x)
def _layernorm(x, axis=-1, eps=1e-5):
    m = x.mean(dim=axis, keepdim=True)
    v = x.var(dim=axis, keepdim=True, unbiased=False)
    return (x - m) / torch.sqrt(v + eps)
def _scatter_add(tgt, idx, src, axis=0):
    return tgt.clone().index_add_(axis, idx, src)
def _segment_sum(src, seg, n):
    out = torch.zeros((n,) + src.shape[1:], dtype=src.dtype)
    return out.index_add_(0, seg, src)
def _pad(x, pw):
    flat = tuple(v for pair in reversed(pw) for v in pair)
    return torch.nn.functional.pad(x, flat)

kernel_name = 'interleave_layers__sliding_local_dilated__ssm_conv_h3__prompt'

def setup_inputs():

    _w = (lambda a: (lambda t: t.to(torch.bfloat16) if t.is_floating_point() else t)(torch.as_tensor(a)))
    def _A_setup():
        B, S, D = 1, 2048, 512
        return {
            "x": _w((lambda *s: torch.randn(*s, dtype=torch.bfloat16) * 0.05)(B, S, D)),
            "wqkv": _w((lambda *s: torch.randn(*s, dtype=torch.bfloat16) * 0.05)(D, 3 * D)),
            "wo": _w((lambda *s: torch.randn(*s, dtype=torch.bfloat16) * 0.05)(D, D)),
        }
    _A_in = _A_setup()

    def _B_setup():
        B, S, D = 1, 2048, 512
        ks = 4
        return {
            "q": _w((lambda *s: torch.randn(*s, dtype=torch.bfloat16) * 0.05)(B, S, D)),
            "k": _w((lambda *s: torch.randn(*s, dtype=torch.bfloat16) * 0.4)(B, S, D)),
            "v": _w((lambda *s: torch.randn(*s, dtype=torch.bfloat16) * 0.4)(B, S, D)),
            "shift_k": _w((lambda *s: torch.randn(*s, dtype=torch.bfloat16) * 0.4)(D, ks) * 0.5),
            "ssm_k": _w((lambda *s: torch.randn(*s, dtype=torch.bfloat16) * 0.8)(D, S) * (1.0 / S ** 0.5)),
            "ssm_D": _w((lambda *s: torch.randn(*s, dtype=torch.bfloat16) * 0.4)(D)),
        }
    _B_in = _B_setup()

    return {
        "A_x": _A_in["x"],
        "A_wqkv": _A_in["wqkv"],
        "A_wo": _A_in["wo"],
        "B_q": _B_in["q"],
        "B_k": _B_in["k"],
        "B_v": _B_in["v"],
        "B_shift_k": _B_in["shift_k"],
        "B_ssm_k": _B_in["ssm_k"],
        "B_ssm_D": _B_in["ssm_D"],
    }

def reference(A_x, A_wqkv, A_wo, B_q, B_k, B_v, B_shift_k, B_ssm_k, B_ssm_D):
    def _A_ref(x, wqkv, wo):
        B, S, D = x.shape
        H = 32; Dh = D // H
        W, dil = 8, 2
        qkv = (lambda x, *d: x.permute(*d))((x @ wqkv).reshape(B, S, 3, H, Dh), 2, 0, 3, 1, 4)
        q, k, v = qkv[0], qkv[1], qkv[2]
        scale = Dh ** -0.5
        pos = torch.arange(S)
        off = torch.arange(W) * dil
        idx = pos[:, None] - off[None, :]
        mask = idx >= 0
        idx_c = torch.where(mask, idx, 0).reshape(-1)
        kg = k[:, :, idx_c, :].reshape(B, H, S, W, Dh)
        vg = v[:, :, idx_c, :].reshape(B, H, S, W, Dh)
        logits = torch.einsum("bhsd,bhswd->bhsw", q, kg) * scale
        logits = torch.where(mask[None, None, :, :], logits, -1e9)
        attn = _softmax(logits, axis=-1)
        o = torch.einsum("bhsw,bhswd->bhsd", attn, vg)
        return (lambda x, *d: x.permute(*d))(o, 0, 2, 1, 3).reshape(B, S, D) @ wo

    def _B_ref(q, k, v, shift_k, ssm_k, ssm_D):
        B, S, D = q.shape
        ks = shift_k.shape[-1]
        k_pad = _pad(k, ((0, 0), (ks - 1, 0), (0, 0)))
        k_shift = torch.zeros_like(k)
        for t in range(ks):
            k_shift = k_shift + k_pad[:, t:t + S, :] * shift_k[:, ks - 1 - t][None, None, :]
        kv = k_shift * v
        L = 2 * S
        kv_pad = _pad(kv, ((0, 0), (0, S), (0, 0)))
        kf_pad = _pad(ssm_k, ((0, 0), (0, S)))
        kv_f = (lambda x, axes=(-2,-1): torch.fft.rfft2(x.float(), dim=axes))(kv_pad[:, None, :, :], axes=(1, 2))
        kf_f = (lambda x, axes=(-2,-1): torch.fft.rfft2(x.float(), dim=axes))(kf_pad[:, None, :], axes=(1, 2))
        kf_f = (lambda x, *d: x.permute(*d))(kf_f, 1, 2, 0)
        y_f = kv_f * kf_f[None, :, :, :]
        y = (lambda x, s, axes=(-2,-1): torch.fft.irfft2(x, s=s, dim=axes).to(torch.bfloat16))(y_f, (1, L), axes=(1, 2))[:, 0, :S, :]
        y = y + kv * ssm_D[None, None, :]
        return q * y

    x = A_x
    x = x + _A_ref(_layernorm(x, axis=-1), A_wqkv, A_wo)
    x = x + _B_ref(_layernorm(x, axis=-1), B_k, B_v, B_shift_k, B_ssm_k, B_ssm_D)
    x = x + _A_ref(_layernorm(x, axis=-1), A_wqkv, A_wo)
    x = x + _B_ref(_layernorm(x, axis=-1), B_k, B_v, B_shift_k, B_ssm_k, B_ssm_D)
    return x
```

```cpp
#include <hip/hip_runtime.h>
#include <hip/hip_bf16.h>
typedef unsigned short u16;
__device__ __forceinline__ float bf2f(u16 v){ return __uint_as_float(((unsigned)v)<<16); }
__device__ __forceinline__ u16 f2bf(float f){ __hip_bfloat16 b = __float2bfloat16(f); return *(u16*)&b; }
__device__ __forceinline__ float rbf(float f){ return bf2f(f2bf(f)); }
constexpr int S=2048, D=512;

__global__ void k_kv(const u16* k, const u16* v, const u16* sk, u16* kv){
  int i = blockIdx.x*256+threadIdx.x; int s=i/D, d=i%D;
  float acc=0.f;
  for(int t=0;t<4;t++){ int ss=s+t-3; float kk = ss>=0? bf2f(k[ss*D+d]):0.f; float p = rbf(kk*bf2f(sk[d*4+3-t])); acc = rbf(acc+p);}
  kv[i]=f2bf(acc*bf2f(v[i]));
}
__global__ void k_conv(const u16* kv, const u16* h, const u16* Dv, u16* Y){
  int i = blockIdx.x*256+threadIdx.x; int s=i/D, d=i%D;
  float acc=0.f;
  for(int t=0;t<=s;t++) acc += bf2f(kv[t*D+d])*bf2f(h[d*S+(s-t)]);
  float y = rbf(acc); float u = rbf(bf2f(kv[i])*bf2f(Dv[d]));
  Y[i]=f2bf(y+u);
}
__global__ void k_ln(const u16* x, u16* xn){
  int s=blockIdx.x, l=threadIdx.x; float v[8]; float sum=0;
  for(int j=0;j<8;j++){ v[j]=bf2f(x[s*D+l*8+j]); sum+=v[j]; }
  for(int o=32;o;o>>=1) sum+=__shfl_xor(sum,o);
  float m=sum/D; float q=0; for(int j=0;j<8;j++){ float t=v[j]-m; q+=t*t; }
  for(int o=32;o;o>>=1) q+=__shfl_xor(q,o);
  float r=rsqrtf(q/D+1e-5f);
  for(int j=0;j<8;j++) xn[s*D+l*8+j]=f2bf((v[j]-m)*r);
}
__global__ void k_gemm(const u16* A, const u16* B, const u16* res, u16* C, int N){
  int n=blockIdx.x*256+threadIdx.x, m=blockIdx.y; float acc=0;
  for(int k=0;k<D;k++) acc+=bf2f(A[m*D+k])*bf2f(B[k*N+n]);
  if(res) acc = rbf(acc)+bf2f(res[m*N+n]);
  C[m*N+n]=f2bf(acc);
}
__global__ void k_attn(const u16* qkv, u16* o){
  int i=blockIdx.x*256+threadIdx.x; int s=i/32, h=i%32;
  float q[16]; for(int j=0;j<16;j++) q[j]=bf2f(qkv[s*1536+h*16+j]);
  float lg[8]; float mx=-1e30f;
  for(int w=0;w<8;w++){ int t=s-2*w; float a=0; if(t>=0){ for(int j=0;j<16;j++) a+=q[j]*bf2f(qkv[t*1536+512+h*16+j]); a*=0.25f;} else a=-1e9f; lg[w]=a; mx=fmaxf(mx,a);}
  float sum=0; for(int w=0;w<8;w++){ lg[w]=__expf(lg[w]-mx); sum+=lg[w]; }
  float out[16]; for(int j=0;j<16;j++) out[j]=0;
  for(int w=0;w<8;w++){ int t=s-2*w; if(t<0) t=0; float p=rbf(lg[w]/sum); for(int j=0;j<16;j++) out[j]+=p*bf2f(qkv[t*1536+1024+h*16+j]); }
  for(int j=0;j<16;j++) o[s*D+h*16+j]=f2bf(out[j]);
}
__global__ void k_gate(const u16* x, const u16* xn, const u16* Y, u16* out){
  int i=blockIdx.x*256+threadIdx.x; out[i]=f2bf(bf2f(x[i])+rbf(bf2f(xn[i])*bf2f(Y[i])));
}
extern "C" void kernel_launch(void* const* d_in, const int* in_sizes, int n_in, void* d_out, int out_size, void* d_ws, size_t ws_size, hipStream_t stream){
  const u16* x=(const u16*)d_in[0]; const u16* wqkv=(const u16*)d_in[1]; const u16* wo=(const u16*)d_in[2];
  const u16* k=(const u16*)d_in[4]; const u16* v=(const u16*)d_in[5]; const u16* sk=(const u16*)d_in[6]; const u16* h=(const u16*)d_in[7]; const u16* Dv=(const u16*)d_in[8];
  u16* ws=(u16*)d_ws; const int N=S*D;
  u16 *kv=ws, *Y=ws+N, *xn=ws+2*N, *o=ws+3*N, *x1=ws+4*N, *x2=ws+5*N, *qkv=ws+6*N;
  k_kv<<<N/256,256,0,stream>>>(k,v,sk,kv);
  k_conv<<<N/256,256,0,stream>>>(kv,h,Dv,Y);
  const u16* xin=x;
  for(int l=0;l<2;l++){
    k_ln<<<S,64,0,stream>>>(xin,xn);
    k_gemm<<<dim3(1536/256,S),256,0,stream>>>(xn,wqkv,nullptr,qkv,1536);
    k_attn<<<S*32/256,256,0,stream>>>(qkv,o);
    k_gemm<<<dim3(512/256,S),256,0,stream>>>(o,wo,xin,x1,512);
    k_ln<<<S,64,0,stream>>>(x1,xn);
    u16* dst = l==0? x2 : (u16*)d_out;
    k_gate<<<N/256,256,0,stream>>>(x1,xn,Y,dst);
    xin=x2;
  }
}
```

```cpp
#include <hip/hip_runtime.h>
#include <hip/hip_bf16.h>

typedef unsigned short u16;
typedef unsigned u32;
typedef __attribute__((ext_vector_type(8))) __bf16 bf16x8;
typedef __attribute__((ext_vector_type(2))) __bf16 bf16x2;
typedef __attribute__((ext_vector_type(4))) short s16x4;
typedef __attribute__((ext_vector_type(4))) float f32x4;
typedef __attribute__((ext_vector_type(2))) float f32x2;
typedef __attribute__((ext_vector_type(4))) u32 u32x4;
typedef __attribute__((ext_vector_type(2))) u32 u32x2;
#define LAS __attribute__((address_space(3)))
#define DEV __device__ __forceinline__

constexpr u32 WS_F = 0, WS_F2 = 32768, WS_ZERO_BYTES = 65536;
constexpr u32 WS_ST1 = 0x20000, WS_ST2 = 0x40000, WS_X2S = 0x60000;
constexpr u32 WS_KVT = 1u << 20, WS_YT = 3u << 20, WS_O1B = 5u << 20, WS_O2B = 7u << 20, WS_X2B = 9u << 20;
constexpr u32 WS_DBG = 16u << 20;
constexpr u32 WS_BYTES = 32u << 20;
constexpr u32 L_A = 0, L_B = 81920;
constexpr u32 L_WO = L_A;
constexpr u32 L_V2 = L_A + 65536;
constexpr u32 L_ST2 = L_A + 75776;
constexpr u32 L_FLG2 = L_A + 81536;
constexpr u32 L_RING = L_B;
constexpr u32 L_TOEP = L_B;
constexpr u32 L_U = L_B + 33280;
constexpr u32 L_V1 = L_B + 51584;
constexpr u32 L_ST1 = L_B + 73728;
constexpr u32 L_FLG1 = L_B + 74752;
constexpr u32 LDS_BYTES = 163840;
#define PF 12
constexpr u32 SPIN_CAP = 1u << 18;
constexpr u32 SENT = 0xFFFFFFFFu, ONE2 = 0x3f803f80u;
constexpr u32 FL_ST = 0, FL_V = 4, FL_O = 20, FL_WO = 24, FL_Y = 28, FL_CV = 32, FL_RD = 40, FL_F = 48;

DEV float bflo(u32 v) { return __uint_as_float(v << 16); }
DEV float bfhi(u32 v) { return __uint_as_float(v & 0xffff0000u); }
DEV u32 pk2(float a, float b) { bf16x2 z; z[0] = (__bf16)a; z[1] = (__bf16)b; return __builtin_bit_cast(u32, z); }
DEV f32x4 mfma32(bf16x8 a, bf16x8 b, f32x4 c) { return __builtin_amdgcn_mfma_f32_16x16x32_bf16(a, b, c, 0, 0, 0); }
DEV f32x4 mfma16(u32x2 a, u32x2 b, f32x4 c) {
  return __builtin_amdgcn_mfma_f32_16x16x16bf16_1k(__builtin_bit_cast(s16x4, a), __builtin_bit_cast(s16x4, b), c, 0, 0, 0); }
DEV float dot2(u32 a, u32 b, float c) {
  return __builtin_amdgcn_fdot2_f32_bf16(__builtin_bit_cast(bf16x2, a), __builtin_bit_cast(bf16x2, b), c, false); }
template <class T> DEV T lds_rd(u32 a) { return *(LAS T*)(size_t)a; }
template <class T> DEV T lds_rdv(u32 a) { return *(volatile LAS T*)(size_t)a; }
template <class T> DEV void lds_wr(u32 a, T v) { *(LAS T*)(size_t)a = v; }
DEV u32x2 lds_tr(u32 a) { return __builtin_bit_cast(u32x2, __builtin_amdgcn_ds_read_tr16_b64_v4i16((LAS s16x4*)(size_t)a)); }
DEV bf16x8 frag(u32x2 lo, u32x2 hi) { u32x4 v = {lo.x, lo.y, hi.x, hi.y}; return __builtin_bit_cast(bf16x8, v); }
DEV bf16x8 tr_frag(u32 a) { return frag(lds_tr(a), lds_tr(a + 512)); }
DEV void glds16(const void* sbase, u32 voff, u32 lds_dst) {
  u32 keep;
  asm volatile("s_mov_b32 %0, m0\n\ts_mov_b32 m0, %3\n\ts_nop 0\n\tglobal_load_lds_dwordx4 %1, %2\n\ts_mov_b32 m0, %0"
               : "=&s"(keep) : "v"(voff), "s"(sbase), "s"(lds_dst) : "memory");
}
template <int N> DEV void vmwait() { asm volatile("s_waitcnt vmcnt(%0)" :: "n"(N) : "memory"); }
template <int N> DEV void bar_lgkm() { asm volatile("s_waitcnt lgkmcnt(%0)\n\ts_barrier" :: "n"(N) : "memory"); }
#define FENCE() asm volatile("" ::: "memory")
#define SCHED() __builtin_amdgcn_sched_barrier(0)
#define SCHED_DS() __builtin_amdgcn_sched_barrier(0x2 | 0x4 | 0x8)
#define SGB(m, n) __builtin_amdgcn_sched_group_barrier(m, n, 0)
DEV u32 opaque(u32 x) { asm volatile("" : "+v"(x)); return x; }
DEV float xq_add(float x) {
  auto s = __builtin_amdgcn_permlane16_swap(__float_as_uint(x), __float_as_uint(x), false, false);
  x = __uint_as_float(s[0]) + __uint_as_float(s[1]);
  auto t = __builtin_amdgcn_permlane32_swap(__float_as_uint(x), __float_as_uint(x), false, false);
  return __uint_as_float(t[0]) + __uint_as_float(t[1]);
}
DEV float xq_max(float x) {
  auto s = __builtin_amdgcn_permlane16_swap(__float_as_uint(x), __float_as_uint(x), false, false);
  x = fmaxf(__uint_as_float(s[0]), __uint_as_float(s[1]));
  auto t = __builtin_amdgcn_permlane32_swap(__float_as_uint(x), __float_as_uint(x), false, false);
  return fmaxf(__uint_as_float(t[0]), __uint_as_float(t[1]));
}
DEV float row0_bcast(float x) {
  auto s = __builtin_amdgcn_permlane16_swap(__float_as_uint(x), __float_as_uint(x), false, false);
  auto t = __builtin_amdgcn_permlane32_swap(s[0], s[0], false, false);
  return __uint_as_float(t[0]);
}
DEV float col0_bcast(float x) {
  return __int_as_float(__builtin_amdgcn_update_dpp(0, __float_as_int(x), 0x150, 0xf, 0xf, false));
}
DEV float ror8_add(float x) { return x + __int_as_float(__builtin_amdgcn_update_dpp(0, __float_as_int(x), 0x128, 0xf, 0xf, false)); }
DEV u32x4 widen(u32x2 a, u32x2 b) {
  auto r0 = __builtin_amdgcn_permlane16_swap(a.x, b.x, false, false);
  auto r1 = __builtin_amdgcn_permlane16_swap(a.y, b.y, false, false);
  u32x4 v = {r0[0], r1[0], r0[1], r1[1]};
  return v;
}
typedef __amdgpu_buffer_rsrc_t rsrc_t;
DEV u32x4 ld16(rsrc_t r, u32 off, u32 so = 0) { return __builtin_amdgcn_raw_buffer_load_b128(r, off, so, 16); }
DEV u32x2 ld8(rsrc_t r, u32 off, u32 so = 0) { return __builtin_amdgcn_raw_buffer_load_b64(r, off, so, 16); }
DEV u32 ld4(rsrc_t r, u32 off, u32 so = 0) { return __builtin_amdgcn_raw_buffer_load_b32(r, off, so, 16); }
DEV void st16(rsrc_t r, u32 off, u32x4 v, u32 so = 0) { __builtin_amdgcn_raw_buffer_store_b128(v, r, off + so, 0, 16); }
DEV void st8(rsrc_t r, u32 off, u32x2 v, u32 so = 0) { __builtin_amdgcn_raw_buffer_store_b64(v, r, off, so, 16); }
DEV void st4(rsrc_t r, u32 off, u32 v) { __builtin_amdgcn_raw_buffer_store_b32(v, r, off, 0, 16); }
DEV u32 mx3(u32 a, u32 b, u32 c) { return max(max(a, b), c); }
DEV u32 mx4(u32x4 v, u32 m) { return mx3(mx3(m, v.x, v.y), v.z, v.w); }
DEV u32x4 sane(u32x4 v) { v.x = min(v.x, SENT - 1); v.y = min(v.y, SENT - 1); v.z = min(v.z, SENT - 1); v.w = min(v.w, SENT - 1); return v; }
DEV bool all_ne(u32 v, u32 k) { return __builtin_amdgcn_ballot_w64(v == k) == 0; }
#define ALD(d, vo, so, imm) asm volatile("buffer_load_dwordx4 %0, %1, %2, %3 offen offset:%4 sc1" : "=v"(d) : "v"(vo), "s"(RS), "s"(so), "n"(imm) : "memory")
#define RLD(d, vo, so, imm) asm volatile("buffer_load_dwordx4 %0, %1, %2, %3 offen offset:%4 sc1" : "+v"(d) : "v"(vo), "s"(RS), "s"(so), "n"(imm) : "memory")
DEV void wait_flags(rsrc_t r, u32 off, u32 so) {
#pragma nounroll
  for (u32 sp = 0; sp < SPIN_CAP; ++sp) {
    FENCE();
    u32 v = ld4(r, off, so);
    if (all_ne(v, 0)) break;
    __builtin_amdgcn_s_sleep(1);
  }
  FENCE();
}
DEV void lds_signal(u32 a) {
  u32 va = a, one = 1;
  asm volatile("s_mov_b64 exec, 1\n\tds_add_u32 %0, %1\n\ts_mov_b64 exec, -1" :: "v"(va), "v"(one) : "memory");
}
DEV u32 lds_arrive(u32 a) {
  u32 va = a, one = 1, old;
  asm volatile("s_mov_b64 exec, 1\n\tds_add_rtn_u32 %0, %1, %2\n\ts_mov_b64 exec, -1\n\ts_waitcnt lgkmcnt(0)"
               : "=&v"(old) : "v"(va), "v"(one) : "memory");
  return __builtin_amdgcn_readfirstlane(old);
}
DEV u32 lds_flag(u32 a) { return lds_rdv<u32>(a); }
DEV void lds_wait(u32 a, u32 n) {
#pragma nounroll
  for (u32 sp = 0; sp < (SPIN_CAP << 4); ++sp) {
    if (__builtin_amdgcn_readfirstlane(lds_flag(a)) >= n) break;
  }
  FENCE();
}
#define TS(i) do {} while (0)
#define TSDUMP() do {} while (0)
constexpr int bwait(int j) {
  const int n = j == 0 ? 4 : (j + 5 > 16 ? 16 : j + 5);
  return 3 * (n - (j + 1)) + ((j >= 2 && j <= 6) ? 1 : 0) + (j >= 12 ? 4 : 0) + (j >= 14 ? 6 : 0) + (j >= 15 ? 7 : 0);
}

__global__ void __launch_bounds__(512, 2)
fused_kernel(const u16* __restrict__ x0, const u16* __restrict__ wqkv, const u16* __restrict__ wo,
             const u16* __restrict__ kin, const u16* __restrict__ vin, const u16* __restrict__ shk,
             const u16* __restrict__ hk, const u16* __restrict__ dvec, u16* __restrict__ out, char* ws) {
  const int tid = threadIdx.x, lane = tid & 63;
  const int w = __builtin_amdgcn_readfirstlane(tid >> 6);
  const int c = lane & 15, q = lane >> 4;
  const int b = blockIdx.x;
  const int t = (b & 7) * 4 + (b >> 6), g = (b >> 3) & 7;
  const int s0 = t * 64;
  const int hh = w & 3;
  TS(0);
  asm volatile("" :: "s"(x0), "s"(wqkv), "s"(wo), "s"(kin), "s"(vin), "s"(shk), "s"(hk), "s"(dvec), "s"(out), "s"(ws));
  const u32 x_voff = (u32)(lane >> 2) * 1024u + (((u32)(lane & 3) ^ ((4u - (lane >> 4)) & 3u)) * 16u);
  auto issueX = [&](int jj) {
    const int ks = 4 * jj + hh;
#pragma unroll
    for (int m = 0; m < 5; ++m) {
      int row0 = s0 - 16 + 16 * m; if (row0 < 0) row0 = 0;
      const char* base = (const char*)x0 + (size_t)row0 * 1024 + ks * 64;
      if (m != 0 || lane >= 4) glds16(base, x_voff, L_A + (m * 16 + ks) * 1024);
    }
  };
  const u32 sw_off = (((((u32)lane & 7) >> 1) ^ (((lane >> 4) & 1) | ((hh & 1) << 1))) * 32u) + (lane & 1) * 16u;
  const u32 w_voff = (u32)(lane >> 3) * 3072u + sw_off;
  const char* w_base = (const char*)wqkv + (size_t)hh * 8 * 3072 + g * 128;
  auto issueW = [&](int kc) {
#pragma unroll
    for (int sec = 0; sec < 3; ++sec)
      glds16(w_base + (size_t)kc * 32 * 3072 + sec * 1024, w_voff, L_RING + hh * 1024 + (kc % 6) * 12288 + sec * 4096);
  };
  if (w < 4) { issueX(0); issueX(1); } else { issueW(0); issueW(1); issueW(2); issueW(3); }
  SCHED();
  TS(16);

  const rsrc_t R = __builtin_amdgcn_make_buffer_rsrc((void*)ws, 0, WS_BYTES, 0x00020000);
  const u32x4 RS = {(u32)(size_t)ws, (u32)((size_t)ws >> 32) & 0xffffu, WS_BYTES, 0x00020000u};
  const u32 tr_row = (u32)((lane & 15) >> 2) * 128u + (lane & 3) * 8u;
  const u32 tr_sw = (((lane & 15) >> 3) & 1) | ((q & 1) << 1);
  const u32 a_lane = (u32)lane * 16u;
  const u32 a1_lane = (u32)c * 64u + (((u32)q ^ ((4u - (c >> 2)) & 3u)) * 16u);
  const int tp = t ? t - 1 : 0;
  const int ch = g * 64 + 2 * t + (hh >> 1), hb = hh & 1;
  const u32 tb0 = L_TOEP + (hh >> 1) * (4 * 4160);
  const u32 ub = L_U + hh * 4576;
  const u32 f_off = (lane < 32 ? lane * 8 + g : lane < 40 ? t * 8 + (lane & 7) : tp * 8 + (lane & 7)) * 128;
  const u32x4 ones4 = {SENT, SENT, SENT, SENT};
  const u32 xo = WS_X2B + ((t * 4 + hh) * 16 + 2 * g) * 1024;

  const u32 xs0 = WS_X2B + ((tp * 4 + 3) * 16 + w) * 1024, xs1 = WS_X2B + (t * 64 + w) * 1024;
  auto x2_load = [&](u32x4 (&xv)[5], int half) {
    const u32 vo = a_lane + half * 8192;
    ALD(xv[0], vo, xs0, 0); ALD(xv[1], vo, xs1, 0); ALD(xv[2], vo, xs1 + 16384, 0); ALD(xv[3], vo, xs1 + 32768, 0); ALD(xv[4], vo, xs1 + 49152, 0);
  };
#define X2_WAIT(xv, N) asm volatile("s_waitcnt vmcnt(" #N ")" : "+v"(xv[0]), "+v"(xv[1]), "+v"(xv[2]), "+v"(xv[3]), "+v"(xv[4]) :: "memory")
  auto x2_put = [&](u32x4 (&xv)[5], int half) {
    const u32 vo = a_lane + half * 8192;
#pragma nounroll
    for (u32 sp = 0; sp < SPIN_CAP; ++sp) {
      u32 m = 0;
#pragma unroll
      for (int i = 0; i < 5; ++i) m = mx4(xv[i], m);
      if (all_ne(m, SENT)) break;
      RLD(xv[0], vo, xs0, 0); RLD(xv[1], vo, xs1, 0); RLD(xv[2], vo, xs1 + 16384, 0); RLD(xv[3], vo, xs1 + 32768, 0); RLD(xv[4], vo, xs1 + 49152, 0);
      X2_WAIT(xv, 0);
    }
    const u32 d0 = opaque(L_B + (half * 8 + w) * 1024 + a_lane), d1 = opaque(d0 + 65536);
#pragma unroll
    for (int m = 0; m < 4; ++m) lds_wr<u32x4>(d0 + m * 16384, xv[m]);
    lds_wr<u32x4>(d1, xv[4]);
  };

  if (w < 4) {
    bf16x8 Wq[16], Wk[16];
    f32x4 aq[4], ak[5];
    float csq[4], csk[4];
    const u32 o_off = ((t * 4 + (q & 1)) * 16 + 2 * g + (hh >> 1)) * 1024 + ((2 * (hh & 1) + (q >> 1)) * 16 + c) * 16;
    u32 kk[11], vv[8]; u32x4 shv;
    if (w == 0) lds_wr<u32x4>(L_A + (lane >> 2) * 1024 + (lane & 3) * 16, u32x4{ONE2, ONE2, ONE2, ONE2});
    if (w == 1 && lane < 16) lds_wr<u32>(L_FLG1 + lane * 4, 0u);
#pragma unroll
    for (int i = 0; i < 4; ++i) aq[i] = f32x4{0, 0, 0, 0};
#pragma unroll
    for (int i = 0; i < 5; ++i) ak[i] = f32x4{0, 0, 0, 0};
    float cq = 0.f, cq2 = 0.f;
    float srs[5], smr[5];
    {
      bf16x8 af[2][5];
      u32 pa[4], pb[4];
      const u32 xa0 = opaque(L_A + a1_lane), xa4 = opaque(L_A + 65536 + a1_lane);
      const u32 tr0 = opaque(L_RING + q * 1024 + tr_row + ((hh ^ tr_sw) * 32u)), tr4 = opaque(tr0 + 49152);
#pragma unroll
      for (int j = 0; j <= 16; ++j) {
        if (j == 0) vmwait<5>();
        if (j == 4)
          asm volatile("s_waitcnt vmcnt(10)"
                       : "+v"(kk[0]), "+v"(kk[1]), "+v"(kk[2]), "+v"(kk[3]), "+v"(kk[4]), "+v"(kk[5]), "+v"(kk[6]), "+v"(kk[7]),
                         "+v"(kk[8]), "+v"(kk[9]), "+v"(kk[10]), "+v"(vv[0]), "+v"(vv[1]), "+v"(vv[2]), "+v"(vv[3]), "+v"(vv[4]),
                         "+v"(vv[5]), "+v"(vv[6]), "+v"(vv[7]), "+v"(shv) :: "memory");
        if (j == 8) vmwait<0>();
        if (j == 13) {
          vmwait<0>();
          if (lds_arrive(L_FLG1 + FL_F) == 3) { if (lane == 0) st4(R, WS_F + (t * 8 + g) * 128, 1u); }
        }
        if (j == 0 || j == 16) bar_lgkm<0>(); else bar_lgkm<5>();
        SCHED();
        if ((j & 3) == 0) TS(20 + (j >> 2));
        if (j < 16) {
          const u32 ba = (j % 6 >= 4 ? tr4 + (j % 6 - 4) * 12288 : tr0 + (j % 6) * 12288);
          Wq[j] = tr_frag(ba);
          Wk[j] = tr_frag(ba + 4096);
          SCHED_DS();
#pragma unroll
          for (int m = 0; m < 5; ++m) {
            af[j & 1][m] = m < 4 ? lds_rd<bf16x8>(xa0 + (m * 16 + j) * 1024) : lds_rd<bf16x8>(xa4 + j * 1024);
            SCHED_DS();
          }
        }
        if (j == 15) {
          const u32 st_l = opaque(L_ST1 + c * 4);
#pragma unroll
          for (int m = 0; m < 5; ++m) { srs[m] = lds_rd<float>(st_l + m * 64); smr[m] = lds_rd<float>(st_l + 320 + m * 64); }
        }
        if (j >= 1) {
          const int ks = j - 1;
          u32x4 wv = __builtin_bit_cast(u32x4, Wq[ks]);
          cq = dot2(wv.x, ONE2, cq); cq2 = dot2(wv.y, ONE2, cq2);
          cq = dot2(wv.z, ONE2, cq); cq2 = dot2(wv.w, ONE2, cq2);
          if (j == 16) {
            cq = xq_add(cq + cq2);
#pragma unroll
            for (int r = 0; r < 4; ++r) csq[r] = __int_as_float(__builtin_amdgcn_ds_bpermute((4 * q + r) * 4, __float_as_int(cq)));
            SCHED();
          }
#pragma unroll
          for (int m = 0; m < 5; ++m) {
            ak[m] = mfma32(Wk[ks], af[ks & 1][m], ak[m]);
            if (m) aq[m - 1] = mfma32(Wq[ks], af[ks & 1][m], aq[m - 1]);
          }
        }
        if (j >= 4 && j <= 7) {
          if (j == 4 && s0 + (lane >> 3) == 0) { kk[0] = 0; kk[1] = 0; kk[2] = 0; }
          const float a0 = bflo(shv.x), a1 = bfhi(shv.x), a2 = bflo(shv.y), a3 = bfhi(shv.y);
          const float b0 = bflo(shv.z), b1 = bfhi(shv.z), b2 = bflo(shv.w), b3 = bfhi(shv.w);
          float ua[2], ubv[2];
#pragma unroll
          for (int e = 0; e < 2; ++e) {
            const int i = 2 * (j - 4) + e;
            ua[e] = (bflo(kk[i]) * a3 + bflo(kk[i + 1]) * a2 + bflo(kk[i + 2]) * a1 + bflo(kk[i + 3]) * a0) * bflo(vv[i]);
            ubv[e] = (bfhi(kk[i]) * b3 + bfhi(kk[i + 1]) * b2 + bfhi(kk[i + 2]) * b1 + bfhi(kk[i + 3]) * b0) * bfhi(vv[i]);
          }
          const int src = (((lane & 7) << 3) | (lane >> 3)) << 2;
          pa[j - 4] = __builtin_amdgcn_ds_bpermute(src, pk2(ua[0], ua[1]));
          pb[j - 4] = __builtin_amdgcn_ds_bpermute(src, pk2(ubv[0], ubv[1]));
        }
        if (j >= 1 && j < 16) {
#pragma unroll
          for (int i = 0; i < 9; ++i) { SGB(0x8, 1); SGB(0x100, 1); SGB(0x2, 4); }
        }
        SCHED();
        if (j == 0) {
          const int jo = lane >> 3, cp = (lane & 7) + 8 * w;
          const int sk = s0 + 8 * jo - 3;
#pragma unroll
          for (int i = 0; i < 11; ++i) {
            int row = sk + i; if (row < 0) row = 0;
            const u32 off = (u32)row * 1024u + g * 128u + cp * 4u;
            asm volatile("global_load_dword %0, %1, %2" : "=v"(kk[i]) : "v"(off), "s"(kin) : "memory");
          }
#pragma unroll
          for (int i = 0; i < 8; ++i) {
            const u32 off = (u32)(s0 + 8 * jo + i) * 1024u + g * 128u + cp * 4u;
            asm volatile("global_load_dword %0, %1, %2" : "=v"(vv[i]) : "v"(off), "s"(vin) : "memory");
          }
          const u32 off = (u32)(g * 64 + cp * 2) * 8u;
          asm volatile("global_load_dwordx4 %0, %1, %2" : "=v"(shv) : "v"(off), "s"(shk) : "memory");
        }
        if (j == 1) issueX(2);
        if (j == 2) issueX(3);
        if (j == 8) {
          const u32 off = WS_KVT + (u32)(g * 64 + ((lane >> 3) + 8 * w) * 2) * 4096u + (u32)(s0 + 8 * (lane & 7)) * 2u;
          st16(R, off, u32x4{pa[0], pa[1], pa[2], pa[3]}); st16(R, off + 4096, u32x4{pb[0], pb[1], pb[2], pb[3]});
          st16(R, o_off, ones4, WS_O1B); st16(R, o_off + 32768, ones4, WS_O1B);
          st8(R, ((q & 1) ? WS_ST2 : WS_ST1) + (((t * 4 + w) * 8 + g) * 16 + c) * 8, u32x2{SENT, SENT});
        }
        SCHED();
      }
    }
    TS(2);
#pragma unroll
    for (int r = 0; r < 4; ++r) csk[r] = col0_bcast(ak[0][r]);
    const int par = c & 1;
    const bool own0 = (c - (4 * q + par)) >= 0, own1 = (c - (4 * q + par + 2)) >= 0;

#pragma unroll
    for (int layer = 0; layer < 2; ++layer) {
      const u32 l_v = layer ? L_V2 : L_V1, l_fl = layer ? L_FLG2 : L_FLG1;
      u32x2 Kp[5], Qp[4];
#pragma unroll
      for (int m = 0; m < 5; ++m) {
        Kp[m].x = pk2(ak[m][0] * srs[m] - smr[m] * csk[0], ak[m][1] * srs[m] - smr[m] * csk[1]);
        Kp[m].y = pk2(ak[m][2] * srs[m] - smr[m] * csk[2], ak[m][3] * srs[m] - smr[m] * csk[3]);
        if (m) {
          Qp[m - 1].x = pk2(aq[m - 1][0] * srs[m] - smr[m] * csq[0], aq[m - 1][1] * srs[m] - smr[m] * csq[1]);
          Qp[m - 1].y = pk2(aq[m - 1][2] * srs[m] - smr[m] * csq[2], aq[m - 1][3] * srs[m] - smr[m] * csq[3]);
        }
      }
      f32x4 so[4], sp[4];
#pragma unroll
      for (int i = 0; i < 4; ++i) {
        f32x4 z = {0, 0, 0, 0};
        so[i] = mfma16(Kp[i + 1], Qp[i], z);
        sp[i] = mfma16(Kp[i], Qp[i], z);
      }
      u32x2 Vp[5];
      const u32 v_l = opaque(l_v + hh * 2560 + lane * 8);
      u32 vf = lds_flag(l_fl + FL_V + 4 * hh);
#pragma unroll
      for (int m = 0; m < 5; ++m) Vp[m] = lds_rdv<u32x2>(v_l + m * 512);
      u32x2 Po[4], Pp[4];
#pragma unroll
      for (int i = 0; i < 4; ++i) {
        float o0 = par ? so[i][1] : so[i][0], o1 = par ? so[i][3] : so[i][2];
        float p0 = par ? sp[i][1] : sp[i][0], p1 = par ? sp[i][3] : sp[i][2];
        if (i == 0 && t == 0) { p0 = -1e30f; p1 = -1e30f; }
        float l0 = own0 ? o0 : p0, l1 = own1 ? o1 : p1;
        float mx = xq_max(fmaxf(l0, l1));
        const float sc = 0.25f * 1.44269504088896f;
        float e0 = __builtin_amdgcn_exp2f((l0 - mx) * sc), e1 = __builtin_amdgcn_exp2f((l1 - mx) * sc);
        float inv = __builtin_amdgcn_rcpf(xq_add(e0 + e1));
        u32 pp = pk2(e0 * inv, e1 * inv);
        u32 v0 = (pp & 0xffffu) << (16 * par), v1 = (pp >> 16) << (16 * par);
        Po[i].x = own0 ? v0 : 0u; Pp[i].x = own0 ? 0u : v0;
        Po[i].y = own1 ? v1 : 0u; Pp[i].y = own1 ? 0u : v1;
      }
      if (__builtin_expect(__builtin_amdgcn_readfirstlane(vf) == 0, 0)) {
        lds_wait(l_fl + FL_V + 4 * hh, 1);
#pragma unroll
        for (int m = 0; m < 5; ++m) Vp[m] = lds_rdv<u32x2>(v_l + m * 512);
      }
      u32x2 Op[4];
#pragma unroll
      for (int i = 0; i < 4; ++i) {
        f32x4 z = {0, 0, 0, 0};
        z = mfma16(Vp[i + 1], Po[i], z);
        z = mfma16(Vp[i], Pp[i], z);
        Op[i].x = pk2(z[0], z[1]); Op[i].y = pk2(z[2], z[3]);
      }
      st16(R, o_off, sane(widen(Op[0], Op[1])), layer ? WS_O2B : WS_O1B);
      st16(R, o_off + 32768, sane(widen(Op[2], Op[3])), layer ? WS_O2B : WS_O1B);
      lds_signal(l_fl + FL_O);
      TS(layer ? 11 : 3);
      if (layer) { TSDUMP(); __builtin_amdgcn_endpgm(); }
      st16(R, o_off, ones4, WS_O2B); st16(R, o_off + 32768, ones4, WS_O2B);
      lds_wait(L_FLG1 + FL_CV + (hh >> 1) * 4, 2);
      TS(5);
      f32x4 cy[8]; u32 f2v;
#pragma unroll
      for (int i = 0; i < 8; ++i) cy[i] = f32x4{0, 0, 0, 0};
      {
        const u32 f_lane = opaque(tb0 + (3 - (c & 3)) * 4160 + 2 * (2028 - 4 * (c >> 2) + 8 * q) - 64 * (hb * 4) - 64 * 59);
        const u32 g_lane = opaque(ub + 2 * (240 - 16 + 16 * c + 8 * q) - 64 * (hb * 4) - 64 * 3);
#pragma unroll
        for (int rr = 0; rr < 4; ++rr) {
          if (rr == 3) { FENCE(); f2v = ld4(R, f_off, WS_F2); FENCE(); }
          bf16x8 F[8];
#pragma unroll
          for (int i = 0; i < 8; ++i) {
            const u32 o = 64 * (59 - (8 * i + rr));
            F[i] = frag(lds_rdv<u32x2>(f_lane + o), lds_rdv<u32x2>(f_lane + o + 8));
          }
#pragma unroll
          for (int n = 0; n < 8; ++n) {
            bf16x8 G = lds_rd<bf16x8>(g_lane + 64 * (3 - rr) + 512 * n);
#pragma unroll
            for (int mm = 0; n + mm < 8; ++mm) cy[mm + n] = mfma32(F[mm], G, cy[mm + n]);
          }
        }
      }
      TS(6);
      {
        u32x2 uu[4];
        const u32 u_l = opaque(ub + 480 + 2 * (1024 * hb + 16 * c + 4 * q));
#pragma unroll
        for (int i = 0; i < 4; ++i) uu[i] = lds_rdv<u32x2>(u_l + 512 * i);
        FENCE();
        f32x4 mine[4];
        const u32 r_l = opaque(ub + a_lane);
        if (hb == 0) {
#pragma unroll
          for (int i = 0; i < 4; ++i) { lds_wr<f32x4>(r_l + i * 1024, cy[4 + i]); mine[i] = cy[i]; }
        } else {
#pragma unroll
          for (int i = 0; i < 4; ++i) { lds_wr<f32x4>(r_l + i * 1024, cy[i]); mine[i] = cy[4 + i]; }
        }
        lds_signal(L_FLG1 + FL_RD + (hh >> 1) * 4);
        const float dd = bflo((u32)dvec[ch]);
#pragma unroll
        for (int i = 0; i < 4; ++i) {
          mine[i][0] += bflo(uu[i].x) * dd; mine[i][1] += bfhi(uu[i].x) * dd;
          mine[i][2] += bflo(uu[i].y) * dd; mine[i][3] += bfhi(uu[i].y) * dd;
        }
        lds_wait(L_FLG1 + FL_WO, 4);
        lds_wait(L_FLG1 + FL_RD + (hh >> 1) * 4, 2);
        const u32 pbb = opaque(L_U + (hh ^ 1) * 4576 + a_lane);
        f32x4 o[4];
#pragma unroll
        for (int i = 0; i < 4; ++i) o[i] = lds_rd<f32x4>(pbb + i * 1024);
        u32x2 yp[4];
#pragma unroll
        for (int i = 0; i < 4; ++i) {
          yp[i].x = pk2(mine[i][0] + o[i][0], mine[i][1] + o[i][1]);
          yp[i].y = pk2(mine[i][2] + o[i][2], mine[i][3] + o[i][3]);
        }
        const u32 yo = 2 * (256 * (4 * hb + (q & 1)) + 16 * c + 8 * (q >> 1));
        st16(R, yo, sane(widen(yp[0], yp[1])), WS_YT + (u32)ch * 4096u);
        st16(R, yo + 1024, sane(widen(yp[2], yp[3])), WS_YT + (u32)ch * 4096u);
      }
      TS(7);
      if (!all_ne(f2v, 0)) wait_flags(R, f_off, WS_F2);
      FENCE();
      {
        u32x4 yv[2];
        const u32 y_off = (u32)(lane >> 3) * 4096u + (lane & 7) * 16, y_so = WS_YT + (u32)(g * 64 + w * 16) * 4096u + s0 * 2;
#pragma nounroll
        for (u32 sp = 0; sp < SPIN_CAP; ++sp) {
          FENCE();
          yv[0] = ld16(R, y_off, y_so); yv[1] = ld16(R, y_off, y_so + 8 * 4096);
          if (all_ne(mx4(yv[1], mx4(yv[0], 0)), SENT)) break;
        }
        FENCE();
        TS(8);
        const u32 y_l = L_V2 + (w * 16 + (lane >> 3)) * 160 + (lane & 7) * 16;
        lds_wr<u32x4>(y_l, yv[0]); lds_wr<u32x4>(y_l + 8 * 160, yv[1]);
        vmwait<0>();
        lds_signal(L_FLG1 + FL_Y);
      }
      bar_lgkm<0>();
      u32x4 xv0[5], xv1[5];
      x2_load(xv0, 0); x2_load(xv1, 1);
      X2_WAIT(xv0, 5);
      x2_put(xv0, 0);
      bar_lgkm<0>();
      TS(10);
#pragma unroll
      for (int i = 0; i < 4; ++i) aq[i] = f32x4{0, 0, 0, 0};
#pragma unroll
      for (int i = 0; i < 5; ++i) ak[i] = f32x4{0, 0, 0, 0};
      const u32 xb0 = opaque(L_B + a_lane), xb4 = opaque(L_B + 65536 + a_lane);
#pragma unroll
      for (int ks = 0; ks < 16; ++ks) {
        if (ks == 4) { X2_WAIT(xv1, 0); x2_put(xv1, 1); }
        if (ks == 8) {
          bar_lgkm<0>();
          const u32 st_l = opaque(L_ST2 + c * 4);
#pragma unroll
          for (int m = 0; m < 5; ++m) { srs[m] = lds_rd<float>(st_l + m * 64); smr[m] = lds_rd<float>(st_l + 320 + m * 64); }
        }
#pragma unroll
        for (int m = 0; m < 5; ++m) {
          bf16x8 a = m < 4 ? lds_rd<bf16x8>(xb0 + (m * 16 + ks) * 1024) : lds_rd<bf16x8>(xb4 + ks * 1024);
          ak[m] = mfma32(Wk[ks], a, ak[m]);
          if (m) aq[m - 1] = mfma32(Wq[ks], a, aq[m - 1]);
        }
      }
      TS(12);
    }
  } else {
    const int wb = hh;
    bf16x8 Wv[16];
    f32x4 av[5];
    u32x4 hA[2]; u32x2 hB[2];
    const char* wo_base = (const char*)wo + (size_t)wb * 8192 + g * 128;
    const u32 wo_voff = (u32)(lane >> 3) * 1024u + sw_off;
    auto issueWo = [&](int ks) { glds16(wo_base + (size_t)ks * 32768, wo_voff, L_WO + wb * 16384 + ks * 1024); };
#pragma unroll
    for (int i = 0; i < 5; ++i) av[i] = f32x4{0, 0, 0, 0};
    u32x2 res[4]; u32 fv, dummy;
    f32x4 vrs[5], vmr[5];
    {
      bf16x8 af[2][5], sf[2][2];
      f32x4 s_sum = {0, 0, 0, 0}, s_sq = {0, 0, 0, 0}, t_sum = {0, 0, 0, 0}, t_sq = {0, 0, 0, 0};
      const u32x4 one4 = {ONE2, ONE2, ONE2, ONE2};
      const bf16x8 onef = __builtin_bit_cast(bf16x8, one4);
      const u32 xa0 = opaque(L_A + a1_lane), xa4 = opaque(L_A + 65536 + a1_lane), xs = opaque(L_A + (wb + 1) * 16384 + a1_lane);
      const u32 xt = opaque(L_A + wb * 4096 + a1_lane);
      const u32 tr0 = opaque(L_RING + 8192 + q * 1024 + tr_row + ((hh ^ tr_sw) * 32u)), tr4 = opaque(tr0 + 49152);
      bf16x8 tf[4];
#pragma unroll
      for (int j = 0; j <= 16; ++j) {
        switch (j) {
#define BW(J) case J: vmwait<bwait(J)>(); break;
          BW(0)
          BW(1) BW(2) BW(3) BW(4) BW(5) BW(6) BW(8) BW(9) BW(10) BW(11) BW(12) BW(13) BW(14) BW(15)
          case 7: asm volatile("s_waitcnt vmcnt(%1)" : "+v"(dummy) : "n"(bwait(7)) : "memory"); break;
          default: break;
        }
        if (j == 0 || j == 16 || j == 13 || j == 14) bar_lgkm<0>(); else bar_lgkm<5>();
        SCHED();
        if (j < 16) {
          const u32 ba = (j % 6 >= 4 ? tr4 + (j % 6 - 4) * 12288 : tr0 + (j % 6) * 12288);
          Wv[j] = tr_frag(ba);
          SCHED_DS();
#pragma unroll
          for (int m = 0; m < 5; ++m) {
            af[j & 1][m] = m < 4 ? lds_rd<bf16x8>(xa0 + (m * 16 + j) * 1024) : lds_rd<bf16x8>(xa4 + j * 1024);
            SCHED_DS();
          }
        }
        if (j >= 4 && j <= 11) { sf[j & 1][0] = lds_rd<bf16x8>(xs + (2 * (j - 4)) * 1024); sf[j & 1][1] = lds_rd<bf16x8>(xs + (2 * (j - 4) + 1) * 1024); }
        if (j == 8) {
#pragma unroll
          for (int i = 0; i < 4; ++i) tf[i] = lds_rd<bf16x8>(xt + i * 1024);
        }
        if (j == 9) {
          const u32 fq = (4u - (c >> 2)) & 3u;
          const u32 r_l = opaque(L_A + ((wb + 1) * 16 + 2 * g) * 1024 + c * 64 + (q & 1) * 8);
          const u32 p0 = (((u32)(q >> 1)) ^ fq) * 16u, p1 = ((2u + (q >> 1)) ^ fq) * 16u;
          res[0] = lds_rd<u32x2>(r_l + p0); res[1] = lds_rd<u32x2>(r_l + p1);
          res[2] = lds_rd<u32x2>(r_l + p0 + 1024); res[3] = lds_rd<u32x2>(r_l + p1 + 1024);
        }
        if (j == 15) {
          const u32 st_l = opaque(L_ST1 + q * 16);
#pragma unroll
          for (int m = 0; m < 5; ++m) { vrs[m] = lds_rd<f32x4>(st_l + m * 64); vmr[m] = lds_rd<f32x4>(st_l + 320 + m * 64); }
        }
        if (j >= 1) {
          const int ks = j - 1;
#pragma unroll
          for (int m = 0; m < 5; ++m) av[m] = mfma32(af[ks & 1][m], Wv[ks], av[m]);
        }
        if (j >= 5 && j <= 12) {
#pragma unroll
          for (int e = 0; e < 2; ++e) { s_sum = mfma32(onef, sf[(j - 1) & 1][e], s_sum); s_sq = mfma32(sf[(j - 1) & 1][e], sf[(j - 1) & 1][e], s_sq); }
        }
        if (j >= 9 && j <= 12) { t_sum = mfma32(onef, tf[j - 9], t_sum); t_sq = mfma32(tf[j - 9], tf[j - 9], t_sq); }
        if (j >= 1 && j < 16) {
#pragma unroll
          for (int i = 0; i < 5; ++i) { SGB(0x8, 1); SGB(0x100, 2); }
        }
        if (j == 12) {
          const bool r1 = c & 1, r2 = c & 2;
          float sq = r2 ? (r1 ? s_sq[3] : s_sq[2]) : (r1 ? s_sq[1] : s_sq[0]);
          float tq = r2 ? (r1 ? t_sq[3] : t_sq[2]) : (r1 ? t_sq[1] : t_sq[0]);
          const float mu = s_sum[0] * (1.f / 512.f);
          const float rs = __builtin_amdgcn_rsqf(fmaxf(sq * (1.f / 512.f) - mu * mu, 0.f) + 1e-5f);
          if (q == (c >> 2)) {
            lds_wr<float>(L_ST1 + ((wb + 1) * 16 + c) * 4, rs); lds_wr<float>(L_ST1 + 320 + ((wb + 1) * 16 + c) * 4, mu * rs);
            lds_wr<f32x2>(L_FLG1 + 256 + c * 32 + wb * 8, f32x2{t_sum[0], tq});
          }
        }
        if (j == 13 && wb == 0) {
          f32x4 p0 = lds_rd<f32x4>(L_FLG1 + 256 + c * 32), p1 = lds_rd<f32x4>(L_FLG1 + 256 + c * 32 + 16);
          const float mu = (p0[0] + p0[2] + p1[0] + p1[2]) * (1.f / 512.f);
          const float rs = __builtin_amdgcn_rsqf(fmaxf((p0[1] + p0[3] + p1[1] + p1[3]) * (1.f / 512.f) - mu * mu, 0.f) + 1e-5f);
          if (lane < 16) { lds_wr<float>(L_ST1 + c * 4, rs); lds_wr<float>(L_ST1 + 320 + c * 4, mu * rs); }
        }
        SCHED();
        if (j == 0) { issueW(4); issueW(5); }
        if (j >= 1 && j + 5 <= 15) issueW(j + 5);
        if (j == 1) {
          const char* p = lane < 12 ? ws + (size_t)lane * (1u << 20) + (b * 4 + wb) * 128 : lane == 12 ? (const char*)wo + (b * 4 + wb) * 128
                        : lane == 13 ? (const char*)hk + (size_t)ch * 4096 : (const char*)out + (size_t)s0 * 1024 + (g * 4 + wb) * 128;
          asm volatile("global_load_dword %0, %1, off" : "=v"(dummy) : "v"(p) : "memory");
        }
        if (j == 11) {
#pragma unroll
          for (int i = 0; i < 2; ++i) {
            const int mo = (hb * 2 + i) * 64 + lane;
            const u32 offA = (u32)ch * 4096u + (u32)(2040 - 8 * mo) * 2u;
            const u32 offB = mo == 255 ? offA : offA - 8u;
            asm volatile("global_load_dwordx4 %0, %1, %2" : "=v"(hA[i]) : "v"(offA), "s"(hk) : "memory");
            asm volatile("global_load_dwordx2 %0, %1, %2" : "=v"(hB[i]) : "v"(offB), "s"(hk) : "memory");
          }
        }
        if (j == 13) {
#pragma unroll
          for (int ks = 0; ks < 6; ++ks) issueWo(ks);
        }
        if (j == 14) {
#pragma unroll
          for (int ks = 6; ks < 13; ++ks) issueWo(ks);
        }
        if (j == 15) {
          issueWo(13);
          const char* fp = ws + WS_F;
          asm volatile("global_load_dword %0, %1, %2 sc1" : "=v"(fv) : "v"(f_off), "s"(fp) : "memory");
        }
        SCHED();
      }
    }
    TS(2);
    const float csv = row0_bcast(av[0][0]);
    u32x2 Yv[4];
    float xr[16];
#pragma unroll
    for (int layer = 0; layer < 2; ++layer) {
      const u32 l_v = layer ? L_V2 : L_V1, l_fl = layer ? L_FLG2 : L_FLG1;
      {
        const u32 v_l = opaque(l_v + hh * 2560 + lane * 8);
#pragma unroll
        for (int m = 0; m < 5; ++m) {
          u32x2 vp;
          vp.x = pk2(av[m][0] * vrs[m][0] - vmr[m][0] * csv, av[m][1] * vrs[m][1] - vmr[m][1] * csv);
          vp.y = pk2(av[m][2] * vrs[m][2] - vmr[m][2] * csv, av[m][3] * vrs[m][3] - vmr[m][3] * csv);
          lds_wr<u32x2>(v_l + m * 512, vp);
        }
      }
      lds_signal(l_fl + FL_V + 4 * hh);
      TS(layer ? 11 : 3);
      if (layer == 0) {
        issueWo(14); issueWo(15);
        asm volatile("s_waitcnt vmcnt(2)" : "+v"(hA[0]), "+v"(hA[1]), "+v"(hB[0]), "+v"(hB[1]), "+v"(fv) :: "memory");
        if (!all_ne(fv, 0)) wait_flags(R, f_off, WS_F);
        FENCE();
        TS(4);
        u32x4 uv[4];
        {
          const char* up = ws + WS_KVT + (size_t)ch * 4096;
#pragma unroll
          for (int i = 0; i < 4; ++i)
            asm volatile("global_load_dwordx4 %0, %1, %2 offset:%3 sc1" : "=v"(uv[i]) : "v"(a_lane), "s"(up), "n"(i * 1024) : "memory");
        }
        st16(R, a_lane, ones4, xo); st16(R, a_lane + 1024, ones4, xo);
        st16(R, a_lane, ones4, WS_YT + (u32)ch * 4096u + hb * 2048); st16(R, a_lane + 1024, ones4, WS_YT + (u32)ch * 4096u + hb * 2048);
        st8(R, c * 8, u32x2{SENT, SENT}, WS_X2S + ((t * 4 + wb) * 8 + g) * 128);
        FENCE();
        if (wb == 0 && lane < 16) lds_wr<u32>(L_FLG2 + lane * 4, 0u);
#pragma unroll
        for (int i = 0; i < 2; ++i) {
          const int mo = (hb * 2 + i) * 64 + lane;
          u32 B0 = hB[i].x, B1 = hB[i].y;
          if (mo == 255) { B0 = 0; B1 = 0; }
          const u32 A0 = hA[i].x, A1 = hA[i].y, A2 = hA[i].z, A3 = hA[i].w;
          auto sw16 = [](u32 v) { return __builtin_amdgcn_alignbit(v, v, 16); };
          auto mix = [](u32 lo, u32 hi) { return (lo & 0xffffu) | (hi & 0xffff0000u); };
          const u32 s3 = sw16(A3), s2 = sw16(A2), s1 = sw16(A1), s0_ = sw16(A0), sm = sw16(B1);
          const u32 m3 = mix(A3, A2), m2 = mix(A2, A1), m1 = mix(A1, A0), m0 = mix(A0, B1), mm = mix(B1, B0);
          const u32 t_l = opaque(tb0 + mo * 16);
          lds_wr<u32x4>(t_l + 0 * 4160, u32x4{s3, s2, s1, s0_});
          lds_wr<u32x4>(t_l + 1 * 4160, u32x4{m3, m2, m1, m0});
          lds_wr<u32x4>(t_l + 2 * 4160, u32x4{s2, s1, s0_, sm});
          lds_wr<u32x4>(t_l + 3 * 4160, u32x4{m2, m1, m0, mm});
        }
        if (hb == 0 && lane < 8) lds_wr<u32x4>(tb0 + (lane >> 1) * 4160 + 4096 + (lane & 1) * 16, u32x4{0, 0, 0, 0});
        if (lane < 30) lds_wr<u32x4>(ub + lane * 16, u32x4{0, 0, 0, 0});
        asm volatile("s_waitcnt vmcnt(5)" : "+v"(uv[0]), "+v"(uv[1]), "+v"(uv[2]), "+v"(uv[3]) :: "memory");
        TS(5);
        {
          const u32 u_l = opaque(ub + 480 + a_lane);
#pragma unroll
          for (int i = 0; i < 4; ++i) lds_wr<u32x4>(u_l + i * 1024, uv[i]);
        }
        lds_signal(L_FLG1 + FL_CV + (hh >> 1) * 4);
        lds_signal(L_FLG1 + FL_ST);
        lds_wait(L_FLG1 + FL_ST, 4);
        TS(6);
      }
      u32 wa[4];
#pragma unroll
      for (int nt = 0; nt < 4; ++nt) wa[nt] = opaque(L_WO + q * 16384 + tr_row + ((nt ^ tr_sw) * 32u));
      bf16x8 wf[64];
#pragma unroll
      for (int i = 0; i < PF; ++i) wf[i] = tr_frag(wa[i & 3] + (i >> 2) * 1024);
      if (layer == 0) {
        vmwait<0>();
        if (lds_arrive(L_FLG1 + FL_WO) == 3) { if (lane == 0) st4(R, WS_F2 + (t * 8 + g) * 128, 1u); }
      }
      const u32 ob = (layer ? WS_O2B : WS_O1B) + (t * 4 + wb) * 16384;
      lds_wait(l_fl + FL_O, 4);
      __builtin_amdgcn_s_setprio(2);
      TS(layer ? 12 : 7);
      u32x4 ov[16];
#define OV4(M, G) M(ov[4 * G], a_lane, ob + G * 4096, 0); M(ov[4 * G + 1], a_lane, ob + G * 4096, 1024); M(ov[4 * G + 2], a_lane, ob + G * 4096, 2048); M(ov[4 * G + 3], a_lane, ob + G * 4096, 3072)
      OV4(ALD, 0); OV4(ALD, 1); OV4(ALD, 2); OV4(ALD, 3);
      f32x4 ac[4];
#pragma unroll
      for (int nt = 0; nt < 4; ++nt) ac[nt] = f32x4{0, 0, 0, 0};
#pragma unroll
      for (int gr = 0; gr < 4; ++gr) {
        asm volatile("s_waitcnt vmcnt(%4)" : "+v"(ov[4 * gr]), "+v"(ov[4 * gr + 1]), "+v"(ov[4 * gr + 2]), "+v"(ov[4 * gr + 3]) : "n"(12 - 4 * gr) : "memory");
#pragma nounroll
        for (u32 sp = 0; sp < SPIN_CAP; ++sp) {
          u32 m = 0;
#pragma unroll
          for (int ks = 4 * gr; ks < 4 * gr + 4; ++ks) m = mx4(ov[ks], m);
          if (all_ne(m, SENT)) break;
          if (gr <= 0) { OV4(RLD, 0); }
          if (gr <= 1) { OV4(RLD, 1); }
          if (gr <= 2) { OV4(RLD, 2); }
          OV4(RLD, 3);
          asm volatile("s_waitcnt vmcnt(%4)" : "+v"(ov[4 * gr]), "+v"(ov[4 * gr + 1]), "+v"(ov[4 * gr + 2]), "+v"(ov[4 * gr + 3]) : "n"(12 - 4 * gr) : "memory");
        }
        SCHED();
#pragma unroll
        for (int i = 16 * gr; i < 16 * gr + 16; ++i) {
          ac[i & 3] = mfma32(wf[i], __builtin_bit_cast(bf16x8, ov[i >> 2]), ac[i & 3]);
          if (i + PF < 64) wf[i + PF] = tr_frag(wa[(i + PF) & 3] + ((i + PF) >> 2) * 1024);
        }
#pragma unroll
        for (int i = 0; i < 16; ++i) { if (16 * gr + i + PF < 64) { SGB(0x8, 1); SGB(0x100, 2); } }
        SCHED();
      }
      float s1 = 0.f, s2 = 0.f;
#pragma unroll
      for (int nt = 0; nt < 4; ++nt) {
        xr[4 * nt + 0] = bflo(res[nt].x) + ac[nt][0]; xr[4 * nt + 1] = bfhi(res[nt].x) + ac[nt][1];
        xr[4 * nt + 2] = bflo(res[nt].y) + ac[nt][2]; xr[4 * nt + 3] = bfhi(res[nt].y) + ac[nt][3];
      }
#pragma unroll
      for (int i = 0; i < 16; ++i) { s1 += xr[i]; s2 += xr[i] * xr[i]; }
      s1 = xq_add(s1); s2 = xq_add(s2);
      const u32 stb = (layer ? WS_ST2 : WS_ST1) + (t * 4 + wb) * 1024;
      if (lane < 16) st8(R, c * 8, u32x2{__float_as_uint(s1), __float_as_uint(s2) & 0x7fffffffu}, stb + g * 128);
      TS(layer ? 13 : 8);
      u32x2 g0, g1;
#pragma nounroll
      for (u32 sp = 0; sp < SPIN_CAP; ++sp) {
        FENCE();
        g0 = ld8(R, q * 256 + c * 8, stb);
        g1 = ld8(R, q * 256 + c * 8 + 128, stb);
        if (all_ne((g0.y | g1.y) >> 31, 1)) break;
      }
      FENCE();
      TS(layer ? 14 : 9);
      if (layer == 0) {
        lds_wait(L_FLG1 + FL_Y, 4);
        const u32 y_l = opaque(L_V2 + (4 * q + ((lane & 15) >> 2)) * 160 + wb * 32 + (lane & 3) * 8);
#pragma unroll
        for (int nt = 0; nt < 4; ++nt) Yv[nt] = lds_tr(y_l + nt * 2560);
        TS(10);
      }
      s1 = xq_add(__uint_as_float(g0.x) + __uint_as_float(g1.x));
      s2 = xq_add(__uint_as_float(g0.y) + __uint_as_float(g1.y));
      const float mu = s1 * (1.f / 512.f);
      const float rs = __builtin_amdgcn_rsqf(fmaxf(s2 * (1.f / 512.f) - mu * mu, 0.f) + 1e-5f);
      const float mr = mu * rs;
      u32x2 xp[4];
#pragma unroll
      for (int nt = 0; nt < 4; ++nt) {
        xr[4 * nt + 0] += (xr[4 * nt + 0] * rs - mr) * bflo(Yv[nt].x);
        xr[4 * nt + 1] += (xr[4 * nt + 1] * rs - mr) * bfhi(Yv[nt].x);
        xr[4 * nt + 2] += (xr[4 * nt + 2] * rs - mr) * bflo(Yv[nt].y);
        xr[4 * nt + 3] += (xr[4 * nt + 3] * rs - mr) * bfhi(Yv[nt].y);
        xp[nt].x = pk2(xr[4 * nt + 0], xr[4 * nt + 1]); xp[nt].y = pk2(xr[4 * nt + 2], xr[4 * nt + 3]);
      }
      u32x4 w0 = widen(xp[0], xp[1]), w1 = widen(xp[2], xp[3]);
      if (layer) {
        u16* op = out + (size_t)(s0 + 16 * wb + c) * 512 + g * 64 + 16 * (q & 1) + 8 * (q >> 1);
        *(u32x4*)op = w0; *(u32x4*)(op + 32) = w1;
        TS(15); TSDUMP();
        __builtin_amdgcn_endpgm();
      }
      const u32 xl = ((2 * (q & 1) + (q >> 1)) * 16 + c) * 16;
      st16(R, xl, sane(w0), xo); st16(R, xl + 1024, sane(w1), xo);
      FENCE();
      asm volatile("s_barrier" ::: "memory");
      u32x4 xv0[5], xv1[5];
      x2_load(xv0, 0); x2_load(xv1, 1);
      s1 = 0.f; s2 = 0.f;
#pragma unroll
      for (int nt = 0; nt < 4; ++nt) {
        res[nt] = xp[nt];
        s1 = dot2(xp[nt].x, ONE2, s1); s1 = dot2(xp[nt].y, ONE2, s1);
        s2 = dot2(xp[nt].x, xp[nt].x, s2); s2 = dot2(xp[nt].y, xp[nt].y, s2);
      }
      s1 = xq_add(s1); s2 = xq_add(s2);
      if (lane < 16) st8(R, c * 8, u32x2{__float_as_uint(s1), __float_as_uint(s2) & 0x7fffffffu}, WS_X2S + ((t * 4 + wb) * 8 + g) * 128);
      X2_WAIT(xv0, 6);
      x2_put(xv0, 0);
      bar_lgkm<0>();
#pragma unroll
      for (int i = 0; i < 5; ++i) av[i] = f32x4{0, 0, 0, 0};
      const u32 xb0 = opaque(L_B + a_lane), xb4 = opaque(L_B + 65536 + a_lane);
      u32x4 pa_, pb_;
      const u32 ps_a = WS_X2S + (t * 4 + wb) * 1024, ps_b = WS_X2S + (tp * 4 + 3) * 1024;
      ALD(pa_, a_lane, ps_a, 0); ALD(pb_, a_lane, ps_b, 0);
#pragma unroll
      for (int ks = 0; ks < 16; ++ks) {
        if (ks == 4) { X2_WAIT(xv1, 3); x2_put(xv1, 1); }
        if (ks == 8) {
          asm volatile("s_waitcnt vmcnt(0)" : "+v"(pa_), "+v"(pb_) :: "memory");
#pragma nounroll
          for (u32 sp = 0; sp < SPIN_CAP; ++sp) {
            if (all_ne((pa_.y | pa_.w | pb_.y | pb_.w) >> 31, 1)) break;
            RLD(pa_, a_lane, ps_a, 0); RLD(pb_, a_lane, ps_b, 0);
            asm volatile("s_waitcnt vmcnt(0)" : "+v"(pa_), "+v"(pb_) :: "memory");
          }
          float v[8] = {__uint_as_float(pa_.x), __uint_as_float(pa_.y), __uint_as_float(pa_.z), __uint_as_float(pa_.w),
                        __uint_as_float(pb_.x), __uint_as_float(pb_.y), __uint_as_float(pb_.z), __uint_as_float(pb_.w)};
#pragma unroll
          for (int i = 0; i < 8; ++i) v[i] = xq_add(ror8_add(v[i]));
          const bool hi = lane & 8;
          const float a1 = hi ? v[4] : v[0], a2 = hi ? v[5] : v[1], b1 = hi ? v[6] : v[2], b2 = hi ? v[7] : v[3];
          const float mua = a1 * (1.f / 512.f), mub = b1 * (1.f / 512.f);
          const float rsa = __builtin_amdgcn_rsqf(fmaxf(a2 * (1.f / 512.f) - mua * mua, 0.f) + 1e-5f);
          const float rsb = __builtin_amdgcn_rsqf(fmaxf(b2 * (1.f / 512.f) - mub * mub, 0.f) + 1e-5f);
          if (lane < 16) {
            const u32 sa = L_ST2 + ((hi ? 0 : wb + 1) * 16 + 2 * (lane & 7)) * 4;
            lds_wr<f32x2>(sa, f32x2{rsa, rsb}); lds_wr<f32x2>(sa + 320, f32x2{mua * rsa, mub * rsb});
          }
          bar_lgkm<0>();
          const u32 st_l = opaque(L_ST2 + q * 16);
#pragma unroll
          for (int m = 0; m < 5; ++m) { vrs[m] = lds_rd<f32x4>(st_l + m * 64); vmr[m] = lds_rd<f32x4>(st_l + 320 + m * 64); }
        }
#pragma unroll
        for (int m = 0; m < 5; ++m) {
          bf16x8 a = m < 4 ? lds_rd<bf16x8>(xb0 + (m * 16 + ks) * 1024) : lds_rd<bf16x8>(xb4 + ks * 1024);
          av[m] = mfma32(a, Wv[ks], av[m]);
        }
      }
    }
  }
}

__global__ void zero_k(u32x4* p) { p[blockIdx.x * 256 + threadIdx.x] = u32x4{0,0,0,0}; }
extern "C" void kernel_launch(void* const* d_in, const int* in_sizes, int n_in, void* d_out, int out_size,
                              void* d_ws, size_t ws_size, hipStream_t stream) {
  (void)hipFuncSetAttribute((const void*)fused_kernel, hipFuncAttributeMaxDynamicSharedMemorySize, LDS_BYTES);
  zero_k<<<16, 256, 0, stream>>>((u32x4*)d_ws);
  fused_kernel<<<256, 512, LDS_BYTES, stream>>>((const u16*)d_in[0], (const u16*)d_in[1], (const u16*)d_in[2],
                                                (const u16*)d_in[4], (const u16*)d_in[5], (const u16*)d_in[6],
                                                (const u16*)d_in[7], (const u16*)d_in[8], (u16*)d_out, (char*)d_ws);
}
```

```cpp
#include <hip/hip_runtime.h>
#include <hip/hip_bf16.h>

typedef unsigned short u16;
typedef unsigned u32;
typedef __attribute__((ext_vector_type(8))) __bf16 bf16x8;
typedef __attribute__((ext_vector_type(2))) __bf16 bf16x2;
typedef __attribute__((ext_vector_type(4))) short s16x4;
typedef __attribute__((ext_vector_type(4))) float f32x4;
typedef __attribute__((ext_vector_type(2))) float f32x2;
typedef __attribute__((ext_vector_type(4))) u32 u32x4;
typedef __attribute__((ext_vector_type(2))) u32 u32x2;
#define LAS __attribute__((address_space(3)))
#define DEV __device__ __forceinline__
#define INL __attribute__((always_inline))

constexpr int FSP = 16;
constexpr int PF = 12, PF2 = 24;
constexpr int H1 = 1, KVX = 4, CL0 = 6, O1_AT = 1;
constexpr int DLY_O = 10, DLY_O1 = 3, DLY_Y = 12, DLY_S = 8, DLY_S1 = 11, DLY_S2 = 9, DLY_X = 22, DLY_XB = 11, OPACE = 3;
constexpr u32 WS_F = 0, WS_FC = 256 * FSP, WS_R = 65536;
constexpr u32 WS_ST1 = 0x20000, WS_ST2 = 0x40000, WS_X2S = 0x60000;
constexpr u32 WS_KVT = 1u << 20, WS_YT = 3u << 20, WS_O1B = 5u << 20, WS_O2B = 7u << 20, WS_X2B = 9u << 20;
constexpr u32 WS_BYTES = 32u << 20;
constexpr u32 L_A = 0, L_B = 81920;
constexpr u32 L_WO = L_A;
constexpr u32 L_V2 = L_A + 65536;
constexpr u32 L_ST2 = L_A + 75776;
constexpr u32 L_FLG2 = L_A + 81536;
constexpr u32 L_RING = L_B;
constexpr u32 L_TOEP = L_B + 20480;
constexpr u32 L_U = L_B;
constexpr u32 L_V1 = L_B + 61440;
constexpr u32 L_ST1 = L_B + 73728;
constexpr u32 L_FLG1 = L_B + 74752;
constexpr u32 LDS_BYTES = 163840;
constexpr u32 SPIN_CAP = 1u << 18;
constexpr u32 SENT = 0xFFFFFFFFu, ONE2 = 0x3f803f80u;
constexpr u32 FL_ST = 0, FL_V = 4, FL_O = 20, FL_WO = 24, FL_Y = 28, FL_CV = 32, FL_RD = 40, FL_F = 48, FL_KV = 52;

DEV float bflo(u32 v) { return __uint_as_float(v << 16); }
DEV float bfhi(u32 v) { return __uint_as_float(v & 0xffff0000u); }
DEV u32 pk2(float a, float b) { bf16x2 z; z[0] = (__bf16)a; z[1] = (__bf16)b; return __builtin_bit_cast(u32, z); }
DEV f32x4 mfma32(bf16x8 a, bf16x8 b, f32x4 c) { return __builtin_amdgcn_mfma_f32_16x16x32_bf16(a, b, c, 0, 0, 0); }
DEV f32x4 mfma16(u32x2 a, u32x2 b, f32x4 c) {
  return __builtin_amdgcn_mfma_f32_16x16x16bf16_1k(__builtin_bit_cast(s16x4, a), __builtin_bit_cast(s16x4, b), c, 0, 0, 0); }
DEV float dot2(u32 a, u32 b, float c) {
  return __builtin_amdgcn_fdot2_f32_bf16(__builtin_bit_cast(bf16x2, a), __builtin_bit_cast(bf16x2, b), c, false); }
template <class T> DEV T lds_rd(u32 a) { return *(LAS T*)(size_t)a; }
template <class T> DEV T lds_rdv(u32 a) { return *(volatile LAS T*)(size_t)a; }
template <class T> DEV void lds_wr(u32 a, T v) { *(LAS T*)(size_t)a = v; }
DEV u32x2 lds_tr(u32 a) { return __builtin_bit_cast(u32x2, __builtin_amdgcn_ds_read_tr16_b64_v4i16((LAS s16x4*)(size_t)a)); }
DEV bf16x8 frag(u32x2 lo, u32x2 hi) { u32x4 v = {lo.x, lo.y, hi.x, hi.y}; return __builtin_bit_cast(bf16x8, v); }
DEV bf16x8 tr_frag(u32 a) { return frag(lds_tr(a), lds_tr(a + 512)); }
template <int LOFF, int IMM> DEV void glds1(const void* sbase, u32 voff, u32 lbase) {
  asm volatile("s_add_u32 m0, %2, %3\n\ts_nop 0\n\tglobal_load_lds_dwordx4 %0, %1 offset:%4"
               :: "v"(voff), "s"(sbase), "s"(lbase), "n"(LOFF - IMM), "n"(IMM) : "memory", "scc");
}
template <int LOFF> DEV void glds3(const void* sbase, u32 voff, u32 lbase) {
  asm volatile("s_add_u32 m0, %2, %3\n\ts_nop 0\n\tglobal_load_lds_dwordx4 %0, %1\n\tglobal_load_lds_dwordx4 %0, %1 offset:1024\n\t"
               "global_load_lds_dwordx4 %0, %1 offset:2048"
               :: "v"(voff), "s"(sbase), "s"(lbase), "n"(LOFF) : "memory", "scc");
}
template <int LOFF, int IMM, u32 VADD> DEV u32 glds1a(const void* sbase, u32 voff, u32 lbase) {
  u32 tmp;
  asm volatile("s_add_u32 m0, %3, %4\n\tv_add_u32 %0, %6, %1\n\tglobal_load_lds_dwordx4 %0, %2 offset:%5"
               : "=&v"(tmp) : "v"(voff), "s"(sbase), "s"(lbase), "n"(LOFF - IMM), "n"(IMM), "n"(VADD) : "memory", "scc");
  return tmp;
}
template <u32 M = ~0u> DEV void touch(const void* sbase, u32 voff) {
  asm volatile("global_load_lds_dword %0, %1" :: "v"(voff & M), "s"(sbase) : "memory");
}
template <int IMM> DEV void glds_next(const void* sbase, u32 voff) {
  asm volatile("global_load_lds_dwordx4 %0, %1 offset:%2" :: "v"(voff), "s"(sbase), "n"(IMM) : "memory");
}
template <int I> struct IC { static constexpr int v = I; };
template <int I, int N, class F> DEV void sfor(F&& f) { if constexpr (I < N) { f(IC<I>{}); sfor<I + 1, N>(f); } }
template <int N> DEV void vmwait() { asm volatile("s_waitcnt vmcnt(%0)" :: "n"(N) : "memory"); }
template <int N> DEV void bar_lgkm() { asm volatile("s_waitcnt lgkmcnt(%0)\n\ts_barrier" :: "n"(N) : "memory"); }
#define FENCE() asm volatile("" ::: "memory")
#define SCHED() __builtin_amdgcn_sched_barrier(0)
#define SGB(m, n) __builtin_amdgcn_sched_group_barrier(m, n, 0)
DEV u32 opaque(u32 x) { asm volatile("" : "+v"(x)); return x; }
DEV float xq_add(float x) {
  auto s = __builtin_amdgcn_permlane16_swap(__float_as_uint(x), __float_as_uint(x), false, false);
  x = __uint_as_float(s[0]) + __uint_as_float(s[1]);
  auto t = __builtin_amdgcn_permlane32_swap(__float_as_uint(x), __float_as_uint(x), false, false);
  return __uint_as_float(t[0]) + __uint_as_float(t[1]);
}
DEV float row0_bcast(float x) {
  auto s = __builtin_amdgcn_permlane16_swap(__float_as_uint(x), __float_as_uint(x), false, false);
  auto t = __builtin_amdgcn_permlane32_swap(s[0], s[0], false, false);
  return __uint_as_float(t[0]);
}
DEV float col0_bcast(float x) {
  return __int_as_float(__builtin_amdgcn_update_dpp(0, __float_as_int(x), 0x150, 0xf, 0xf, false));
}
DEV float ror8_add(float x) { return x + __int_as_float(__builtin_amdgcn_update_dpp(0, __float_as_int(x), 0x128, 0xf, 0xf, false)); }
DEV u32x4 widen(u32x2 a, u32x2 b) {
  auto r0 = __builtin_amdgcn_permlane16_swap(a.x, b.x, false, false);
  auto r1 = __builtin_amdgcn_permlane16_swap(a.y, b.y, false, false);
  u32x4 v = {r0[0], r1[0], r0[1], r1[1]};
  return v;
}
typedef __amdgpu_buffer_rsrc_t rsrc_t;
DEV u32x4 ld16(rsrc_t r, u32 off, u32 so = 0) { return __builtin_amdgcn_raw_buffer_load_b128(r, off, so, 16); }
DEV u32 ld4(rsrc_t r, u32 off, u32 so = 0) { return __builtin_amdgcn_raw_buffer_load_b32(r, off, so, 16); }
DEV void st16(rsrc_t r, u32 off, u32x4 v, u32 so = 0) { __builtin_amdgcn_raw_buffer_store_b128(v, r, off + so, 0, 16); }
DEV void st8(rsrc_t r, u32 off, u32x2 v, u32 so = 0) { __builtin_amdgcn_raw_buffer_store_b64(v, r, off, so, 16); }
DEV void st4(rsrc_t r, u32 off, u32 v) { __builtin_amdgcn_raw_buffer_store_b32(v, r, off, 0, 16); }
DEV u32 mx3(u32 a, u32 b, u32 c) { return max(max(a, b), c); }
DEV u32 mx4(u32x4 v, u32 m) { return mx3(mx3(m, v.x, v.y), v.z, v.w); }
DEV u32x4 sane(u32x4 v) { v.x = min(v.x, SENT - 1); v.y = min(v.y, SENT - 1); v.z = min(v.z, SENT - 1); v.w = min(v.w, SENT - 1); return v; }
DEV bool all_ne(u32 v, u32 k) { return __builtin_amdgcn_ballot_w64(v == k) == 0; }
#define ALD(d, vo, so, imm) asm volatile("buffer_load_dwordx4 %0, %1, %2, %3 offen offset:%4 sc1" : "=v"(d) : "v"(vo), "s"(RS), "s"(so), "n"(imm) : "memory")
#define RLD(d, vo, so, imm) asm volatile("buffer_load_dwordx4 %0, %1, %2, %3 offen offset:%4 sc1" : "+v"(d) : "v"(vo), "s"(RS), "s"(so), "n"(imm) : "memory")
DEV void wait_flags(rsrc_t r, u32 off, u32 so) {
#pragma nounroll
  for (u32 sp = 0; sp < SPIN_CAP; ++sp) {
    FENCE();
    u32 v = ld4(r, off, so);
    if (all_ne(v, 0)) break;
    __builtin_amdgcn_s_sleep(1);
  }
  FENCE();
}
DEV void lds_signal(u32 a) {
  u32 va = a, one = 1;
  asm volatile("s_mov_b64 exec, 1\n\tds_add_u32 %0, %1\n\ts_mov_b64 exec, -1" :: "v"(va), "v"(one) : "memory");
}
DEV u32 lds_flag(u32 a) { return lds_rdv<u32>(a); }
DEV void lds_wait(u32 a, u32 n) {
#pragma nounroll
  for (u32 sp = 0; sp < (SPIN_CAP << 4); ++sp) {
    if (__builtin_amdgcn_readfirstlane(lds_flag(a)) >= n) break;
    __builtin_amdgcn_s_sleep(1);
  }
  FENCE();
}
DEV f32x2 pkfma(f32x2 a, f32x2 b, f32x2 c) { return __builtin_elementwise_fma(a, b, c); }
DEV f32x2 lo2(f32x4 v) { return f32x2{v[0], v[1]}; }
DEV f32x2 hi2(f32x4 v) { return f32x2{v[2], v[3]}; }
DEV f32x2 bc2(float v) { return f32x2{v, v}; }
DEV u32 pk2v(f32x2 v) { return pk2(v[0], v[1]); }
DEV float fopq(float x) { asm volatile("" : "+v"(x)); return x; }
DEV void swap16(float& a, float& b) { auto s = __builtin_amdgcn_permlane16_swap(__float_as_uint(a), __float_as_uint(b), false, false); a = __uint_as_float(s[0]); b = __uint_as_float(s[1]); }
DEV void swap32(float& a, float& b) { auto s = __builtin_amdgcn_permlane32_swap(__float_as_uint(a), __float_as_uint(b), false, false); a = __uint_as_float(s[0]); b = __uint_as_float(s[1]); }
constexpr int slot_of(int kc) { return (kc + 2) % 6; }
__global__ void __launch_bounds__(512, 2)
fused_kernel(const u16* __restrict__ x0, const u16* __restrict__ wqkv, const u16* __restrict__ wo,
             const u16* __restrict__ kin, const u16* __restrict__ vin, const u16* __restrict__ shk,
             const u16* __restrict__ hk, const u16* __restrict__ dvec, u16* out, char* ws) {
  const int tid = threadIdx.x, lane = tid & 63;
  const int w = __builtin_amdgcn_readfirstlane(tid >> 6);
  const int c = lane & 15, q = lane >> 4;
  const int b = blockIdx.x;
  const int t = (b & 7) * 4 + (b >> 6), g = (b >> 3) & 7;
  const int s0 = t * 64;
  const int hh = w & 3;
  unsigned long long pc0; u32 code_len;
  asm volatile("s_getpc_b64 %0\n.Lk_here:\n\ts_mov_b32 %1, .Lk_last-.Lk_here" : "=s"(pc0), "=s"(code_len));
  if (w < 4) {
    const u32 kv_l = (u32)(s0 + 16 * hh + (lane >> 3)) * 1024u + (g ^ KVX) * 128u, l0 = L_A + 12288 + hh * 16384;
    const u32 oa = opaque(kv_l + ((((u32)lane ^ (2 * hh)) & 7u) * 16u)), ob_ = opaque(kv_l + ((((u32)lane ^ (2 * hh + 1)) & 7u) * 16u));
    const u32 oc = opaque(kv_l + 8192u + ((((u32)lane ^ (2 * hh + 2)) & 7u) * 16u)), od = opaque(ob_ + 8192u);
    const u32 oz = opaque((u32)((t ? s0 - 8 : 0) + (lane >> 3)) * 1024u + (g ^ KVX) * 128u + (lane & 7) * 16u);
    const int row = s0 - 16 + 10 * g + (lane < 40 ? lane >> 2 : 0);
    const u32 ox = opaque((u32)(row < 0 ? 0 : row) * 1024u + hh * 256u + (lane & 3) * 64u);
    asm volatile("; role A" :: "s"(x0), "s"(wqkv), "s"(wo), "s"(kin), "s"(vin), "s"(shk), "s"(hk), "s"(dvec), "s"(out), "s"(ws));
    glds1<0, 0>(kin, ob_, l0); glds1<1024, 0>(kin, oc, l0); glds1<2048, 0>(vin, oa, l0); glds1<3072, 0>(vin, od, l0);
    glds1<0, 0>(kin, oz, L_A + 12288 + 65536 + hh * 1024);
    asm volatile("s_mov_b32 m0, %0\n\ts_nop 0" :: "n"(L_FLG1 + 2048) : "memory");
    touch<~64u>(x0, ox);
    asm volatile("; end A" ::: "memory");
  } else {
    const u32 wid = (b >> 3) * 4 + hh, hf = (lane & 1) * 64u;
    const u32 oc = opaque(min((((lane & 7) >> 1) * 128u + wid) * 128u + hf, code_len));
    asm volatile("s_mov_b32 m0, %0\n\ts_nop 0" :: "n"(L_FLG1 + 2048) : "memory");
    touch((const void*)(pc0 & ~63ull), oc);
    const bool sl = lane >= 24 && lane < 48, sl2 = lane >= 8;
    u32 ow = opaque(sl ? (wid * 12 + ((lane - 24) >> 1)) * 1024u + hf : (u32)b * 6144u + hh * 1536u + (lane < 24 ? lane : 0) * 64u);
    u32 oo_ = opaque(sl2 ? (wid * 4 + ((lane >> 1) & 3)) * 1024u + hf : (u32)(b * 16 + hh * 4) * 128u + lane * 64u);
    const u32 oh = opaque((u32)(g * 64 + 2 * t) * 4096u + hh * 2048u + (lane & 31) * 64u);
    asm volatile("; role B" :: "s"(x0), "s"(wqkv), "s"(wo), "s"(kin), "s"(vin), "s"(shk), "s"(hk), "s"(dvec), "s"(out), "s"(ws));
    touch<~64u>(hk, oh);
    touch<~64u>(wqkv, sl ? ow + ((3u - (u32)((size_t)wqkv >> 7)) & 7u) * 128u : ow);
    touch<~64u>(wo, sl2 ? oo_ + ((3u - (u32)((size_t)wo >> 7)) & 7u) * 128u : oo_);
    asm volatile("; end B" ::: "memory");
  }
  asm volatile("" : "+s"(ws), "+s"(out));
  SCHED();
  const u32 x_voff = (u32)(lane >> 2) * 1024u + (((u32)(lane & 3) ^ ((4u - (lane >> 4)) & 3u)) * 16u);
  const char* x_base = (const char*)x0 + (ptrdiff_t)(s0 - 16) * 1024 + hh * 64;
  const u32 x_lds = L_A + hh * 1024;
  const u32 x_voff0 = x_voff + (t == 0 ? 16384u : 0u);
  auto issueX1 = [&](auto I) INL {
    constexpr int jj = decltype(I)::v / 5, m = decltype(I)::v % 5;
    if constexpr (m == 0)
      asm volatile("s_add_u32 m0, %2, %3\n\ts_mov_b64 exec, -16\n\tglobal_load_lds_dwordx4 %0, %1 offset:%4\n\ts_mov_b64 exec, -1"
                   :: "v"(x_voff0), "s"(x_base), "s"(x_lds), "n"(jj * 4096 - jj * 256), "n"(jj * 256) : "memory", "scc");
    else glds1a<m * 16384 + jj * 4096, jj * 256, m * 16384u>(x_base, x_voff, x_lds);
  };
  const u32 sw_off = (((((u32)lane & 7) >> 1) ^ (((lane >> 4) & 1) | ((hh & 1) << 1))) * 32u) + (lane & 1) * 16u;
  const u32 w_voff = (u32)(lane >> 3) * 3072u + sw_off;
  const char* w_base = (const char*)wqkv + (size_t)hh * 8 * 3072 + g * 128;
  const u32 w_lds = L_RING + hh * 3072;
  auto issueW = [&](auto KC) INL { constexpr int kc = decltype(KC)::v; glds3<slot_of(kc) * 12288>(w_base, w_voff + kc * 98304u, w_lds); };
  u32 w_cur;
  auto issueW1 = [&](auto I) INL {
    constexpr int kc = decltype(I)::v / 3, sec = decltype(I)::v % 3;
    if constexpr (sec == 0) w_cur = glds1a<slot_of(kc) * 12288, 0, kc * 98304u>(w_base, w_voff, w_lds);
    else glds_next<sec * 1024>(w_base, w_cur);
  };
  const int ch = g * 64 + 2 * t + (hh >> 1), hb = hh & 1;
  const u32 o_off = ((t * 4 + (q & 1)) * 16 + 2 * g + (hh >> 1)) * 1024 + ((2 * (hh & 1) + (q >> 1)) * 16 + c) * 16;
  const int gk = g ^ KVX;
  const u32 kv_off = WS_KVT + (u32)(gk * 64 + ((lane >> 3) + 8 * hh) * 2) * 4096u + (u32)(s0 + 8 * (lane & 7)) * 2u;
  u32x4 shv; u32x4 hA[2]; u32x2 hB[2];

  const rsrc_t R = __builtin_amdgcn_make_buffer_rsrc((void*)ws, 0, WS_BYTES, 0x00020000);
  const u32x4 RS = {(u32)(size_t)ws, (u32)((size_t)ws >> 32) & 0xffffu, WS_BYTES, 0x00020000u};
  const u32 tr_row = (u32)((lane & 15) >> 2) * 128u + (lane & 3) * 8u;
  const u32 tr_sw = (((lane & 15) >> 3) & 1) | ((q & 1) << 1);
  const u32 a_lane = (u32)lane * 16u;
  const u32 a1_lane = (u32)c * 64u + (((u32)q ^ ((4u - (c >> 2)) & 3u)) * 16u);
  const int tp = __builtin_amdgcn_readfirstlane(t ? t - 1 : 0);
  const u32 tb0 = L_TOEP + (hh >> 1) * (4 * 4160);
  const u32x4 ones4 = {SENT, SENT, SENT, SENT};
  const f32x4 zero4 = {0, 0, 0, 0};

  auto f_arrive = [&]() INL { lds_signal(L_FLG1 + FL_F); };
  const u32 xo = WS_X2B + ((t * 4 + hh) * 16 + 2 * g) * 1024;
  const u32 xs0 = WS_X2B + ((tp * 4 + 3) * 16 + w - 4) * 1024, xs1 = WS_X2B + (t * 64 + w - 4) * 1024;
  auto x2_load = [&](u32x4 (&)[5], int half, int dw = 0) INL {
    const u32 vo = a_lane + half * 8192 + (dw + 4) * 1024, l0 = L_B + (half * 8 + w + dw) * 1024;
#define XD(M, SO) asm volatile("s_add_u32 m0, %3, %4\n\ts_nop 0\n\tbuffer_load_dwordx4 %0, %1, %2 offen sc1 lds" :: "v"(vo), "s"(RS), "s"(SO), "s"(l0), "n"(M * 16384) : "memory", "scc")
    XD(0, xs0); XD(1, xs1); XD(2, xs1 + 16384); XD(3, xs1 + 32768); XD(4, xs1 + 49152);
  };
#define X2_WAIT(xv, N) vmwait<N>()
  auto x2_put = [&](u32x4 (&xv)[5], int half, int dw = 0) INL {
    const u32 vo = a_lane + half * 8192 + (dw + 4) * 1024;
    const u32 d0 = opaque(L_B + (half * 8 + w + dw) * 1024 + a_lane), d1 = opaque(d0 + 65536);
#pragma unroll
    for (int m = 0; m < 4; ++m) xv[m] = lds_rdv<u32x4>(d0 + m * 16384);
    xv[4] = lds_rdv<u32x4>(d1);
    u32 m0 = 0;
#pragma unroll
    for (int i = 0; i < 5; ++i) m0 = mx4(xv[i], m0);
    if (__builtin_expect(!all_ne(m0, SENT), 0)) {
#pragma nounroll
      for (u32 sp = 0; sp < SPIN_CAP; ++sp) {
        FENCE();
        xv[0] = ld16(R, vo, xs0); xv[1] = ld16(R, vo, xs1); xv[2] = ld16(R, vo, xs1 + 16384); xv[3] = ld16(R, vo, xs1 + 32768); xv[4] = ld16(R, vo, xs1 + 49152);
        u32 m = 0;
#pragma unroll
        for (int i = 0; i < 5; ++i) m = mx4(xv[i], m);
        if (all_ne(m, SENT)) break;
      }
#pragma unroll
      for (int m = 0; m < 4; ++m) lds_wr<u32x4>(d0 + m * 16384, xv[m]);
      lds_wr<u32x4>(d1, xv[4]);
      asm volatile("s_waitcnt lgkmcnt(0)" ::: "memory");
    }
  };

  if (w < 4) {
    const rsrc_t R0 = __builtin_amdgcn_make_buffer_rsrc((void*)ws, 0, WS_BYTES, 0x00020000);
    const bool master = b == 0 && w == 0;
    if (master) {
#pragma unroll
      for (int i = 0; i < 8; ++i) st16(R0, WS_F + i * 1024 + lane * 16, u32x4{0, 0, 0, 0});
    }
    if (w == 1) st4(R0, WS_R + (t * 8 + g) * 4, 0u);
    FENCE();
    asm volatile("global_load_dwordx4 %0, %1, %2" : "=v"(shv) : "v"((u32)(gk * 64 + ((lane >> 3) + 8 * hh) * 2) * 8u), "s"(shk) : "memory");
    if (w == 1 && lane < 16) lds_wr<u32>(L_FLG1 + lane * 4, 0u);
    bar_lgkm<0>();
    sfor<0, 5>(issueX1);
    if (master) { vmwait<6>(); st16(R0, WS_R + lane * 16, u32x4{1, 1, 1, 1}); }
    FENCE();
    SCHED();
    const u32 xa0 = opaque(L_A + a1_lane), xa4 = opaque(L_A + 65536 + a1_lane);
    auto afr1 = [&](int m, int ks) INL { return m < 4 ? lds_rd<bf16x8>(xa0 + (m * 16 + ks) * 1024) : lds_rd<bf16x8>(xa4 + ks * 1024); };
    const u32 tr0 = opaque(L_RING + q * 3072 + tr_row + ((hh ^ tr_sw) * 32u));
    const u32 tr5 = opaque(tr0 + 61440);

    bf16x8 Wq[16], Wk[16];
    f32x4 aq[4], ak[5], aq0 = zero4;
    f32x4 csq, csk;
    const u32 ub = L_U + hh * 4576;
    u32 dvr; asm volatile("s_load_dword %0, %1, %2" : "=s"(dvr) : "s"(dvec), "s"((ch >> 1) * 4) : "memory");
    if (w == 0 && lane < 48) lds_wr<u32x4>(L_A + (lane >> 2) * 1024 + (lane & 3) * 16, u32x4{ONE2, ONE2, ONE2, ONE2});
#pragma unroll
    for (int i = 0; i < 4; ++i) aq[i] = zero4;
#pragma unroll
    for (int i = 0; i < 5; ++i) ak[i] = zero4;
    const int par = c & 1;
    const bool own0 = (c - (4 * q + par)) >= 0, own1 = (c - (4 * q + par + 2)) >= 0;
    float nmu[5], rq[4], rk0[4], rk1[4];
    u32 so0, sp0, so1, sp1, sa_, sk0, sk1; float finf;
    auto load_stats = [&](u32 l_st, bool rd) INL {
      const u32 ln = opaque(lane), kq = l_st + ((ln >> 4) * 4 + (ln & 1)) * 4;
      const u32 z4 = opaque(0x0c0c0c0cu), selL = par ? 0x05040c0cu : 0x0c0c0504u, selH = par ? 0x07060c0cu : 0x0c0c0706u;
      so0 = own0 ? selL : z4; sp0 = own0 ? z4 : selL; so1 = own1 ? selH : z4; sp1 = own1 ? z4 : selH;
      finf = fopq(__builtin_inff());
      sa_ = opaque(l_st + (ln & 15) * 4); sk0 = opaque(own0 ? kq + 64 : kq); sk1 = opaque(own1 ? kq + 72 : kq + 8);
      if (!rd) {
        const u32 a = L_ST2 + 640 + hh * 128 + (ln >> 4) * 32; csq = lds_rdv<f32x4>(a); csk = lds_rdv<f32x4>(a + 16);
      }
    };
    f32x2 f01[4];
    auto mk_f01 = [&](int i) INL { const float s = rq[i] * (0.25f * 1.44269504088896f); f01[i] = f32x2{rk0[i] * s, rk1[i] * s}; asm volatile("" : "+v"(f01[i])); };
    bool go_ok = true;
    {
      bf16x8 af[2][5];
      auto wadr = [&](int k) INL { return slot_of(k) == 5 ? tr5 : tr0 + slot_of(k) * 12288; };
      u32x2 wt[8];
      u32 gov = 0;
      const u32 c_o = opaque(o_off + WS_O1B), c_y = opaque(a_lane + WS_YT + (u32)ch * 4096u + hb * 2048);
      const u32 c_s = opaque(((q & 1) ? WS_ST2 : WS_ST1) + (((t * 4 + hh) * 8 + g) * 16 + c) * 8);
      u32x4 sent4 = ones4; asm volatile("" : "+v"(sent4));
      auto bcast = [&]() INL { if (master) st16(R, WS_R + a_lane, u32x4{1, 1, 1, 1}); FENCE(); };
      auto half = [&](auto H) INL {
        constexpr int h = decltype(H)::v;
        constexpr int tw = h == 1 ? 2 : ((h & 1) == 0 && h >= 2 && h <= 12) ? h + 2 : -1;
        constexpr int xb = h <= 3 ? 10 + (h >> 1) * 5 + (h & 1) * 3 : -1;
        sfor<0, 9>([&](auto I) INL {
          constexpr int i = decltype(I)::v, m = (i + 1) >> 1;
          if constexpr (i == 0 || (i & 1)) ak[m] = mfma32(Wk[h], af[h & 1][m], ak[m]); else aq[m - 1] = mfma32(Wq[h], af[h & 1][m], aq[m - 1]);
          constexpr int ia = h == 1 ? i - 4 : i, it = h == 1 ? i : i - 5;
          if constexpr (h < 15 && ia >= 0 && ia < 5) af[(h + 1) & 1][ia] = afr1(ia, h + 1);
          if constexpr (tw >= 0 && it >= 0 && it < 4) {
            const u32 ba = wadr(tw + (it >> 1)) + (it & 1) * 1024;
            wt[2 * it] = lds_tr(ba); wt[2 * it + 1] = lds_tr(ba + 512);
          }
          if constexpr (xb >= 0 && (i == 1 || i == 4 || (i == 7 && (h & 1) == 0))) issueX1(IC<(xb >= 0 ? xb : 0) + i / 3>{});
          if constexpr (i == 8) aq0 = mfma32(Wq[h], af[h & 1][0], aq0);
          if constexpr (i == 4) {
            if constexpr (h == CL0) st16(R, c_o, sent4);
            if constexpr (h == CL0 + 1) st16(R, c_o + 32768, sent4);
            if constexpr (h == CL0 + 2) st16(R, c_y, sent4);
            if constexpr (h == CL0 + 3) st16(R, c_y + 1024, sent4);
          }
          if constexpr (i == 7 && h == CL0) st8(R, c_s, u32x2{sent4.x, sent4.y});
          if constexpr (h == 11 && i == 6) load_stats(L_ST1, true);
          if constexpr (i >= 5) {
            constexpr int e = i - 5;
            if constexpr (h == 13) { nmu[e] = lds_rdv<float>(sa_ + 320 + e * 64); rq[e] = lds_rdv<float>(sa_ + 64 + e * 64); }
            if constexpr (h == 14) { rk0[e] = lds_rdv<float>(sk0 + e * 64); rk1[e] = lds_rdv<float>(sk1 + e * 64); }
            if constexpr (h == 14 && e == 3) nmu[4] = lds_rdv<float>(sa_ + 320 + 256);
          }
          SCHED();
        });
        if constexpr (tw >= 0) {
          Wq[tw] = frag(wt[0], wt[1]); Wk[tw] = frag(wt[2], wt[3]); Wq[tw + 1] = frag(wt[4], wt[5]); Wk[tw + 1] = frag(wt[6], wt[7]);
        }
      };
      {
        u32 kk[11], vv[8], pa[4], pb[4];
        const int jo = lane & 7, cp = (lane >> 3) + 8 * hh;
        auto kblk = [&](int i) INL { return i == 0 ? L_A + 12288 + 65536 : L_A + 12288 + ((i - 1) >> 1) * 16384 + ((i - 1) & 1) * 1024; };
        const u32 ia = ((cp & 3) << 2), ch4 = cp >> 2;
        const u32 k1 = opaque(kblk(jo + 1) + ((ch4 ^ (jo + 1)) & 7) * 16 + ia), k0 = opaque(kblk(jo) + ((ch4 ^ jo) & 7) * 16 + ia);
        const u32 v0 = opaque(L_A + 12288 + 2048 + (jo >> 1) * 16384 + (jo & 1) * 1024 + ((ch4 ^ jo) & 7) * 16 + ia);
        const u32 kmask = opaque(s0 + jo == 0 ? 0u : ~0u), kv_o = opaque(kv_off);
        asm volatile("s_waitcnt vmcnt(5)" : "+v"(shv) :: "memory");
        lds_signal(L_FLG1 + FL_KV);
        sfor<5, 10>(issueX1);
        lds_wait(L_FLG1 + FL_KV, 4);
        bcast();
#pragma unroll
        for (int i = 0; i < 3; ++i) kk[i] = lds_rdv<u32>(k0 + (5 + i) * 128);
#pragma unroll
        for (int i = 0; i < 8; ++i) { kk[3 + i] = lds_rdv<u32>(k1 + i * 128); vv[i] = lds_rdv<u32>(v0 + i * 128); }
      f32x2 tap[4], kf[11];
      auto up2 = [&](u32 v) INL { f32x2 r = {bflo(v), bfhi(v)}; asm volatile("" : "+v"(r)); return r; };
      auto kvp = [&](int p) INL {
        if (p == 0) {
          tap[0] = f32x2{bflo(shv.x), bflo(shv.z)}; tap[1] = f32x2{bfhi(shv.x), bfhi(shv.z)};
          tap[2] = f32x2{bflo(shv.y), bflo(shv.w)}; tap[3] = f32x2{bfhi(shv.y), bfhi(shv.w)};
#pragma unroll
          for (int i = 0; i < 3; ++i) kf[i] = up2(kk[i] & kmask);
        }
        const int i = 2 * p;
        kf[i + 3] = up2(kk[i + 3]); kf[i + 4] = up2(kk[i + 4]);
        const f32x2 va = up2(vv[i]), vb = up2(vv[i + 1]);
        f32x2 ua = kf[i] * tap[3], ub_ = kf[i + 1] * tap[3];
        ua = pkfma(kf[i + 1], tap[2], ua); ub_ = pkfma(kf[i + 2], tap[2], ub_);
        ua = pkfma(kf[i + 2], tap[1], ua); ub_ = pkfma(kf[i + 3], tap[1], ub_);
        ua = pkfma(kf[i + 3], tap[0], ua); ub_ = pkfma(kf[i + 4], tap[0], ub_);
        ua *= va; ub_ *= vb;
        pa[p] = pk2(ua[0], ub_[0]); pb[p] = pk2(ua[1], ub_[1]);
      };
#pragma unroll
        for (int p = 0; p < 4; ++p) kvp(p);
        SCHED();
        st16(R, kv_o, u32x4{pa[0], pa[1], pa[2], pa[3]}); st16(R, kv_o + 4096, u32x4{pb[0], pb[1], pb[2], pb[3]});
        if (w == 1) asm volatile("buffer_load_dword %0, %1, %2, 0 offen sc1" : "+v"(gov) : "v"(WS_R + (t * 8 + g) * 4), "s"(RS) : "memory");
      }
      vmwait<7>();
      asm volatile("s_waitcnt lgkmcnt(0)\n\ts_barrier" : "+s"(dvr) :: "memory");
      lds_wr<u32>(L_A + (12 + hh) * 1024 + c * 4, ONE2);
      Wk[0] = tr_frag(wadr(0) + 1024);
#pragma unroll
      for (int m = 0; m < 5; ++m) { af[0][m] = afr1(m, 0); if (m == 1) Wq[0] = tr_frag(wadr(0)); SCHED(); }
      Wq[1] = tr_frag(wadr(1)); Wk[1] = tr_frag(wadr(1) + 1024);
      SCHED();
      half(IC<0>{});
      bar_lgkm<5>();
      SCHED();
      half(IC<1>{});
      sfor<1, 8>([&](auto DD) INL {
        constexpr int d = decltype(DD)::v;
        if (d == 1) vmwait<7>();
        if (d == 3) {
          asm volatile("s_waitcnt vmcnt(0)" : "+v"(gov) :: "memory");
          if (w == 1) { go_ok = __builtin_amdgcn_readfirstlane(gov) != 0; if (go_ok) f_arrive(); }
        }
        if (d == 6) vmwait<0>();
        if (d >= 4 && d <= 6 && __builtin_expect(!go_ok, 0)) {
          if (all_ne(ld4(R, WS_R + (t * 8 + g) * 4), 0)) { go_ok = true; f_arrive(); st4(R, WS_F + (g * 32 + t) * FSP, 1u); }
        }
        if (d == 3 || d == 5) bcast();
        if (d < 7) bar_lgkm<5>();
        SCHED();
        if ((d == 3 || d == 6) && w == 1 && go_ok) st4(R, (d == 3 ? WS_F : WS_FC) + (g * 32 + t) * FSP, 1u);
        SCHED();
        half(IC<2 * d>{});
        if constexpr (d == 7) {
          bar_lgkm<0>();
          SCHED();
#pragma unroll
          for (int m = 0; m < 5; ++m) { ak[m] = mfma32(Wk[15], af[1][m], ak[m]); if (m) aq[m - 1] = mfma32(Wq[15], af[1][m], aq[m - 1]); else aq0 = mfma32(Wq[15], af[1][0], aq0); if (m < 4) mk_f01(m); SCHED(); }
        } else {
          SCHED();
          half(IC<2 * d + 1>{});
        }
      });
    }
#pragma unroll
    for (int r = 0; r < 4; ++r) { csk[r] = col0_bcast(ak[0][r]); csq[r] = col0_bcast(aq0[r]); }
    auto fmx = [&](float a, float b_) INL { return __builtin_amdgcn_fmed3f(a, b_, finf); };

    auto attention = [&](u32 l_v, u32 l_fl, u32 obase) INL {
      u32x2 Kp[5], Qp[4];
#pragma unroll
      for (int m = 0; m < 5; ++m) {
        f32x2 a = pkfma(lo2(csk), bc2(nmu[m]), lo2(ak[m])), b_ = pkfma(hi2(csk), bc2(nmu[m]), hi2(ak[m]));
        f32x2 e = {0, 0}, f = {0, 0};
        if (m) { e = pkfma(lo2(csq), bc2(nmu[m]), lo2(aq[m - 1])); f = pkfma(hi2(csq), bc2(nmu[m]), hi2(aq[m - 1])); }
        SCHED();
        Kp[m].x = pk2v(a); Kp[m].y = pk2v(b_);
        if (m) { Qp[m - 1].x = pk2v(e); Qp[m - 1].y = pk2v(f); }
        SCHED();
      }
      f32x4 so[4], sp[4];
#pragma unroll
      for (int i = 0; i < 4; ++i) { so[i] = mfma16(Kp[i + 1], Qp[i], zero4); sp[i] = mfma16(Kp[i], Qp[i], zero4); }
      u32x2 Vp[5];
      const u32 v_l = opaque(l_v + hh * 2560 + lane * 8);
      u32 vf = lds_flag(l_fl + FL_V + 4 * hh);
#pragma unroll
      for (int m = 0; m < 5; ++m) Vp[m] = lds_rdv<u32x2>(v_l + m * 512);
      SCHED();
      f32x2 l[4]; float mx[4];
#pragma unroll
      for (int i = 0; i < 4; ++i) {
        const float a0 = own0 ? so[i][0] : sp[i][0], a1 = own0 ? so[i][1] : sp[i][1];
        const float a2 = own1 ? so[i][2] : sp[i][2], a3 = own1 ? so[i][3] : sp[i][3];
        const float b0 = fopq(a0), b1 = fopq(a1), b2 = fopq(a2), b3 = fopq(a3);
        l[i] = f32x2{par ? b1 : b0, par ? b3 : b2};
      }
      SCHED();
#pragma unroll
      for (int i = 0; i < 4; ++i) l[i] *= f01[i];
      if (t == 0) { l[0][0] = own0 ? l[0][0] : -1e30f; l[0][1] = own1 ? l[0][1] : -1e30f; }
      SCHED();
#pragma unroll
      for (int i = 0; i < 4; ++i) mx[i] = fmx(l[i][0], l[i][1]);
      SCHED();
      swap16(mx[0], mx[1]); swap16(mx[2], mx[3]);
      float za = fmx(mx[0], mx[1]), zb = fmx(mx[2], mx[3]);
      swap32(za, zb);
      za = fmx(za, zb); zb = za;
      swap32(za, zb);
      mx[0] = za; mx[1] = za; mx[2] = zb; mx[3] = zb;
      swap16(mx[0], mx[1]); swap16(mx[2], mx[3]);
      SCHED();
#pragma unroll
      for (int i = 0; i < 4; ++i) l[i] -= bc2(mx[i]);
      SCHED();
#pragma unroll
      for (int i = 0; i < 4; ++i) { l[i][0] = __builtin_amdgcn_exp2f(l[i][0]); l[i][1] = __builtin_amdgcn_exp2f(l[i][1]); }
      SCHED();
      u32 pp[4]; float sm[4];
#pragma unroll
      for (int i = 0; i < 4; ++i) { sm[i] = l[i][0] + l[i][1]; l[i] *= f32x2{rk0[i], rk1[i]}; }
      SCHED();
#pragma unroll
      for (int i = 0; i < 4; ++i) pp[i] = pk2v(l[i]);
      swap16(sm[0], sm[1]); swap16(sm[2], sm[3]);
      SCHED();
      u32x2 Po[4], Pp[4];
#pragma unroll
      for (int i = 0; i < 4; ++i) {
        Po[i].x = __builtin_amdgcn_perm(pp[i], 0u, so0); Po[i].y = __builtin_amdgcn_perm(pp[i], 0u, so1);
        Pp[i].x = __builtin_amdgcn_perm(pp[i], 0u, sp0); Pp[i].y = __builtin_amdgcn_perm(pp[i], 0u, sp1);
      }
      za = sm[0] + sm[1]; zb = sm[2] + sm[3];
      SCHED();
      if (__builtin_expect(__builtin_amdgcn_readfirstlane(vf) == 0, 0)) {
        lds_wait(l_fl + FL_V + 4 * hh, 1);
#pragma unroll
        for (int m = 0; m < 5; ++m) Vp[m] = lds_rdv<u32x2>(v_l + m * 512);
      }
      f32x4 z[4];
#pragma unroll
      for (int i = 0; i < 4; ++i) z[i] = mfma16(Vp[i + 1], Po[i], zero4);
      swap32(za, zb);
      za = __builtin_amdgcn_rcpf(za + zb); zb = za;
      SCHED();
#pragma unroll
      for (int i = 0; i < 4; ++i) z[i] = mfma16(Vp[i], Pp[i], z[i]);
      swap32(za, zb);
      sm[0] = za; sm[1] = za; sm[2] = zb; sm[3] = zb;
      swap16(sm[0], sm[1]); swap16(sm[2], sm[3]);
      SCHED();
      u32x2 Op[4];
#pragma unroll
      for (int i = 0; i < 4; ++i) { Op[i].x = pk2v(lo2(z[i]) * bc2(sm[i])); Op[i].y = pk2v(hi2(z[i]) * bc2(sm[i])); }
      st16(R, o_off, sane(widen(Op[0], Op[1])), obase);
      st16(R, o_off + 32768, sane(widen(Op[2], Op[3])), obase);
    };
    attention(L_V1, L_FLG1, WS_O1B);
    { const u32 a = L_ST2 + 640 + hh * 128 + q * 32; lds_wr<f32x4>(a, csq); lds_wr<f32x4>(a + 16, csk); }
    if (__builtin_expect(!go_ok, 0)) { wait_flags(R, WS_R + (t * 8 + g) * 4, 0); f_arrive(); st4(R, WS_F + (g * 32 + t) * FSP + (lane & 1) * WS_FC, 1u); }
    const u32 f_lane = opaque(tb0 + (3 - (c & 3)) * 4160 + 2 * (2028 - 4 * (c >> 2) + 8 * q) - 64 * (hb * 4) - 64 * 59);
    const u32 g_lane = opaque(ub + 2 * (240 - 16 + 16 * c + 8 * q) - 64 * (hb * 4) - 64 * 3);
    u32x4 fdv;
    { const u32 dbits = (hb == 0 && q == 2 + (c >> 3)) ? (((ch & 1) ? dvr >> 16 : dvr & 0xffffu) << (16 * (c & 1))) : 0u; const int dw = (c & 7) >> 1;
      fdv = u32x4{dw == 0 ? dbits : 0u, dw == 1 ? dbits : 0u, dw == 2 ? dbits : 0u, dw == 3 ? dbits : 0u}; asm volatile("" : "+v"(fdv)); }
    bool cv_rdy = true;
#pragma nounroll
    for (u32 sp = 0; sp < (SPIN_CAP << 4); ++sp) {
      if (__builtin_amdgcn_readfirstlane(lds_flag(L_FLG1 + FL_CV + (hh >> 1) * 4)) >= 2) break;
      if (sp == DLY_O1) { lds_signal(L_FLG1 + FL_O); cv_rdy = false; }
      if (master && (sp & 15) == 15) st16(R, WS_R + a_lane, u32x4{1, 1, 1, 1});
    }
    FENCE();
    __builtin_amdgcn_s_setprio(1);
    f32x4 cy[8]; u32 r_l, pbb, yo;
#pragma unroll
    for (int i = 0; i < 8; ++i) cy[i] = zero4;
    {
      auto ldF = [&](int rr, int i) INL { const u32 o = 64 * (59 - (8 * i + rr)); return frag(lds_rdv<u32x2>(f_lane + o), lds_rdv<u32x2>(f_lane + o + 8)); };
      auto ldG = [&](int rr, int n) INL { return lds_rdv<bf16x8>(g_lane + 64 * (3 - rr) + 512 * n); };
      bf16x8 F[2][8], G[4]; u32x2 fl[2];
      G[0] = ldG(0, 0);
#pragma unroll
      for (int i = 7; i >= 0; --i) F[0][i] = ldF(0, i);
      G[1] = ldG(0, 1); G[2] = ldG(0, 2);
#pragma unroll
      for (int rr = 0; rr < 4; ++rr) {
#pragma unroll
        for (int n = 0; n < 8; ++n) {
          const int s = rr * 8 + n;
          if (s == 26) { r_l = opaque(ub + a_lane); pbb = opaque(L_U + (hh ^ 1) * 4576 + a_lane);
            yo = opaque(WS_YT + (u32)ch * 4096u + 2 * (256 * (4 * hb + (q & 1)) + 16 * c + 8 * (q >> 1))); }
          if (s == O1_AT && cv_rdy) lds_signal(L_FLG1 + FL_O);
          SCHED();
#pragma unroll
          for (int mm = 7 - n; mm >= 0; --mm) {
            const int k = 7 - n - mm;
            cy[mm + n] = mfma32(F[rr & 1][mm], G[s % 4], cy[mm + n]);
            if (k == 0 && s + 3 < 32) G[(s + 3) % 4] = ldG((s + 3) >> 3, (s + 3) & 7);
            if (rr < 3) {
              const int fa = n <= 4 ? 7 - n : -1, fb = (n >= 1 && n <= 3) ? 3 - n : -1;
              const u32 oa = 64 * (59 - (8 * fa + rr + 1)), ob_ = 64 * (59 - (8 * fb + rr + 1));
              if (fa >= 0 && k == 1) fl[0] = lds_rdv<u32x2>(f_lane + oa);
              if (fa >= 0 && k == 2) { F[(rr + 1) & 1][fa] = frag(fl[0], lds_rdv<u32x2>(f_lane + oa + 8)); }
              if (fb >= 0 && k == 3) fl[1] = lds_rdv<u32x2>(f_lane + ob_);
              if (fb >= 0 && k == 4) { F[(rr + 1) & 1][fb] = frag(fl[1], lds_rdv<u32x2>(f_lane + ob_ + 8)); }
            }
            SCHED();
          }
          if (rr == 0) cy[n] = mfma32(__builtin_bit_cast(bf16x8, fdv), G[s % 4], cy[n]);
        }
      }
    }
    SCHED();
    {
      auto fin = [&](auto HB) INL {
        constexpr int mine = decltype(HB)::v * 4, other = 4 - mine;
        asm volatile("; conv finish %0" :: "n"(mine) : "memory");
#pragma unroll
        for (int i = 0; i < 4; ++i) lds_wr<f32x4>(r_l + i * 1024, cy[other + i]);
        lds_signal(L_FLG1 + FL_RD + (hh >> 1) * 4);
        f32x4 o[4];
#pragma nounroll
        for (u32 sp = 0; sp < (SPIN_CAP << 4); ++sp) {
          const u32 f1 = lds_flag(L_FLG1 + FL_RD + (hh >> 1) * 4);
#pragma unroll
          for (int i = 0; i < 4; ++i) o[i] = lds_rdv<f32x4>(pbb + i * 1024);
          if (__builtin_amdgcn_readfirstlane(f1) >= 2) break;
        }
        u32x2 yp[4];
#pragma unroll
        for (int i = 0; i < 4; ++i) { yp[i].x = pk2v(lo2(cy[mine + i]) + lo2(o[i])); yp[i].y = pk2v(hi2(cy[mine + i]) + hi2(o[i])); }
        st16(R, yo, sane(widen(yp[0], yp[1])));
        st16(R, yo + 1024, sane(widen(yp[2], yp[3])));
        asm volatile("; conv done %0" :: "n"(mine) : "memory");
      };
      if (hb) fin(IC<1>{}); else fin(IC<0>{});
    }
    st16(R, o_off, ones4, WS_O2B); st16(R, o_off + 32768, ones4, WS_O2B);
    if (master) {
#pragma nounroll
      for (u32 sp = 0; sp < SPIN_CAP; ++sp) {
        FENCE();
        const u32x4 f = {ld4(R, WS_F + lane * FSP), ld4(R, WS_F + lane * FSP, 64 * FSP), ld4(R, WS_F + lane * FSP, 128 * FSP), ld4(R, WS_F + lane * FSP, 192 * FSP)};
        if (all_ne(min(min(f.x, f.y), min(f.z, f.w)), 0)) break;
        st16(R, WS_R + a_lane, u32x4{1, 1, 1, 1});
        __builtin_amdgcn_s_sleep(4);
      }
    }
    lds_wait(L_FLG1 + FL_WO, 4);
    if (!master) __builtin_amdgcn_s_sleep(DLY_Y);
    FENCE();
    {
      u32x4 yv[2];
      const u32 y_off = (u32)(lane >> 3) * 4096u + (lane & 7) * 16, y_so = WS_YT + (u32)(g * 64 + w * 16) * 4096u + s0 * 2;
#pragma nounroll
      for (u32 sp = 0; sp < SPIN_CAP; ++sp) {
        FENCE();
        yv[0] = ld16(R, y_off, y_so); yv[1] = ld16(R, y_off, y_so + 8 * 4096);
        if (all_ne(mx4(yv[1], mx4(yv[0], 0)), SENT)) break;
      }
      FENCE();
      const u32 y_l = L_V2 + (w * 16 + (lane >> 3)) * 160 + (lane & 7) * 16;
      lds_wr<u32x4>(y_l, yv[0]); lds_wr<u32x4>(y_l + 8 * 160, yv[1]);
      vmwait<0>();
      lds_signal(L_FLG2 + FL_Y);
    }
    __builtin_amdgcn_s_setprio(0);
    load_stats(L_ST2, false);
    lds_wait(L_FLG2 + FL_Y, 4);
    lds_wait(L_FLG2 + FL_ST, 4);
    __builtin_amdgcn_s_sleep(DLY_X);
    u32x4 xv0[5], xv1[5];
    x2_load(xv0, H1);
    X2_WAIT(xv0, 0);
    x2_put(xv0, H1);
    bar_lgkm<0>();
#pragma unroll
    for (int i = 0; i < 4; ++i) aq[i] = zero4;
#pragma unroll
    for (int i = 0; i < 5; ++i) ak[i] = zero4;
    const u32 xb0 = opaque(L_B + a_lane), xb4 = opaque(L_B + 65536 + a_lane);
    {
      bf16x8 af[2][5];
      auto afr2 = [&](int m, int ks) INL { return m < 4 ? lds_rd<bf16x8>(xb0 + (m * 16 + ks) * 1024) : lds_rd<bf16x8>(xb4 + ks * 1024); };
#pragma unroll
      for (int m = 0; m < 5; ++m) af[0][m] = afr2(m, H1 * 8);
      SCHED();
      sfor<0, 16>([&](auto H) INL {
        constexpr int h = decltype(H)::v;
        constexpr int ks = h ^ (H1 * 8), kn = (h + 1) ^ (H1 * 8);
        if constexpr (h == 7) { asm volatile("s_barrier" ::: "memory"); SCHED(); }
        sfor<0, 9>([&](auto I) INL {
          constexpr int i = decltype(I)::v, m = (i + 1) >> 1;
          if constexpr (i == 0 || (i & 1)) ak[m] = mfma32(Wk[ks], af[h & 1][m], ak[m]); else aq[m - 1] = mfma32(Wq[ks], af[h & 1][m], aq[m - 1]);
          if constexpr (h < 15 && i < 5) af[(h + 1) & 1][i] = afr2(i, kn);
          if constexpr (i >= 5) {
            constexpr int e = i - 5;
            if constexpr (h == 9) nmu[e] = lds_rdv<float>(sa_ + 320 + e * 64);
            if constexpr (h == 10) rq[e] = lds_rdv<float>(sa_ + 64 + e * 64);
            if constexpr (h == 11) rk0[e] = lds_rdv<float>(sk0 + e * 64);
            if constexpr (h == 12) rk1[e] = lds_rdv<float>(sk1 + e * 64);
            if constexpr (h == 13 && e == 0) nmu[4] = lds_rdv<float>(sa_ + 320 + 256);
            if constexpr (h == 15) mk_f01(e);
          }
          SCHED();
        });
      });
    }
    SCHED();
    attention(L_V2, L_FLG2, WS_O2B);
    __builtin_amdgcn_s_sleep(DLY_O);
    lds_signal(L_FLG2 + FL_O);
    asm volatile(".Lk_last:");
    __builtin_amdgcn_endpgm();
  } else {
    {
      const u32 h_a = (u32)ch * 4096u + (u32)(2040 - 8 * (hb * 128 + 64 + lane)) * 2u;
      const u32 h_b = hb * 128 + 64 + lane == 255 ? h_a : h_a - 8u;
      asm volatile("global_load_dwordx4 %0, %1, %2 offset:1024" : "=v"(hA[0]) : "v"(h_a), "s"(hk) : "memory");
      asm volatile("global_load_dwordx2 %0, %1, %2 offset:1016" : "=v"(hB[0]) : "v"(h_a), "s"(hk) : "memory");
      asm volatile("global_load_dwordx4 %0, %1, %2" : "=v"(hA[1]) : "v"(h_a), "s"(hk) : "memory");
      asm volatile("global_load_dwordx2 %0, %1, %2" : "=v"(hB[1]) : "v"(h_b), "s"(hk) : "memory");
    }
    asm volatile("s_barrier" ::: "memory");
    SCHED();
    const u32 xa0 = opaque(L_A + a1_lane), xa4 = opaque(L_A + 65536 + a1_lane);
    auto afr1 = [&](int m, int ks) INL { return m < 4 ? lds_rd<bf16x8>(xa0 + (m * 16 + ks) * 1024) : lds_rd<bf16x8>(xa4 + ks * 1024); };
    const u32 tr0 = opaque(L_RING + q * 3072 + tr_row + ((hh ^ tr_sw) * 32u));
    const u32 tr5 = opaque(tr0 + 61440);

    const int wb = hh;
    bf16x8 Wv[16];
    f32x4 av[5];
    const char* wo_base = (const char*)wo + (size_t)wb * 8192 + g * 128;
    const u32 wo_voff = (u32)(lane >> 3) * 1024u + sw_off, wo_lds = L_WO + wb * 16384;
    auto issueWo = [&](auto KS) INL { constexpr int ks = decltype(KS)::v; glds1a<ks * 1024, 0, ks * 32768u>(wo_base, wo_voff, wo_lds); };
#pragma unroll
    for (int i = 0; i < 5; ++i) av[i] = zero4;
    u32x2 res[4]; u32 fv, fk, fcnt; u32x4 uv[4];
    const u32 us = WS_KVT + (u32)ch * 4096u;
    const u32 fk_off = opaque(WS_F + (gk * 32 + (lane & 31)) * FSP);
    const u32 f_off = opaque(WS_F + (lane < 32 ? g * 32 + lane : lane < 40 ? (lane & 7) * 32 + t : (lane & 7) * 32 + tp) * FSP);
    u32x4 ts[2], tm[2], ts2[2], tm2[2];
    const u32 t_l = opaque(tb0 + (hb * 128 + lane) * 16);
    f32x4 vnm[5];
    {
      bf16x8 af[2][5], sf[2], tf;
      f32x4 s_sum = zero4, s_sq = zero4, t_sum = zero4, t_sq = zero4;
      u32x4 one4 = {ONE2, ONE2, ONE2, ONE2};
      asm volatile("" : "+v"(one4));
      const bf16x8 onef = __builtin_bit_cast(bf16x8, one4);
      const u32 xs = opaque(L_A + (wb + 1) * 16384 + a1_lane);
      const u32 xt = opaque(L_A + wb * 1024 + a1_lane);
      auto trV = [&](auto K) INL { constexpr int k = decltype(K)::v; Wv[k] = tr_frag((slot_of(k) == 5 ? tr5 : tr0 + slot_of(k) * 12288) + 2048); };
      const u32 fq = (4u - (c >> 2)) & 3u, r_l = L_A + ((wb + 1) * 16 + 2 * g) * 1024 + c * 64 + (q & 1) * 8;
      const u32 r_0 = opaque(r_l + (((u32)(q >> 1)) ^ fq) * 16u), r_1 = opaque(r_l + ((2u + (q >> 1)) ^ fq) * 16u);
      const bool dg = q == (c >> 2);
      const u32 p_w = opaque(dg ? L_FLG1 + 256 + c * 32 + wb * 8 : L_FLG1 + 1024 + lane * 8), p_r = opaque(L_FLG1 + 256 + c * 32);
      const u32 s_w = opaque(L_ST1 + ((dg ? wb + 1 : 0) * 16 + c) * 4), st_l = opaque(L_ST1 + 320 + q * 16);
      auto diag = [&](f32x4 v) INL { const float e0 = fopq(v[0]), e1 = fopq(v[1]), e2 = fopq(v[2]), e3 = fopq(v[3]);
        const float lo = (c & 1) ? e1 : e0, hi = (c & 1) ? e3 : e2; return (c & 2) ? hi : lo; };
      auto half = [&](auto H) INL {
        constexpr int h = decltype(H)::v;
        constexpr int sr = h <= 3 ? 2 * h : (h >= 6 && h <= 9) ? 2 * h - 4 : -1;
        constexpr bool sm = (h >= 1 && h <= 4) || (h >= 7 && h <= 10);
        constexpr int wb0 = h == 0 ? -1 : h == 1 ? 18 : h <= 9 ? 24 + 3 * (h - 2) : -1, wn = h == 1 ? 2 : 1;
        auto MV = [&](int m) INL { av[m] = mfma32(af[h & 1][m], Wv[h], av[m]); SCHED(); };
        auto RA = [&](int m) INL { if constexpr (h < 15) { af[(h + 1) & 1][m] = afr1(m, h + 1); } };
        auto DW = [&](auto I) INL { if constexpr (wb0 >= 0) { sfor<0, wn>([&](auto E) INL { issueW1(IC<(wb0 >= 0 ? wb0 : 0) + decltype(I)::v * wn + decltype(E)::v>{}); }); } };
        if constexpr (h == 1) { trV(IC<2>{}); trV(IC<3>{}); SCHED(); }
        MV(0); RA(0); RA(1); DW(IC<0>{}); SCHED();
        MV(1); RA(2); RA(3); SCHED();
        MV(2); RA(4); DW(IC<1>{}); SCHED();
        MV(3);
        if constexpr ((h & 1) == 0 && h >= 2 && h <= 12) { trV(IC<(h <= 12 ? h + 2 : 0)>{}); trV(IC<(h <= 12 ? h + 3 : 0)>{}); }
        SCHED();
        MV(4); DW(IC<2>{}); SCHED();
        if constexpr (sm) {
          s_sum = mfma32(onef, sf[0], s_sum); s_sq = mfma32(sf[0], sf[0], s_sq);
          s_sum = mfma32(onef, sf[1], s_sum); s_sq = mfma32(sf[1], sf[1], s_sq);
        }
        if constexpr (h == 5 || h == 6 || h == 8 || h == 9) { t_sum = mfma32(onef, tf, t_sum); t_sq = mfma32(tf, tf, t_sq); }
        SCHED();
        if constexpr (sr >= 0) { sf[0] = lds_rd<bf16x8>(xs + sr * 1024); sf[1] = lds_rd<bf16x8>(xs + (sr + 1) * 1024); }
        if constexpr (h == 4) tf = lds_rd<bf16x8>(xt);
        if constexpr (h == 5) tf = lds_rd<bf16x8>(xt + 4096);
        if constexpr (h == 7) tf = lds_rd<bf16x8>(xt + 8192);
        if constexpr (h == 8) tf = lds_rd<bf16x8>(xt + 12288);
        if constexpr (h == 10) {
          res[0] = lds_rd<u32x2>(r_0); res[1] = lds_rd<u32x2>(r_1); res[2] = lds_rd<u32x2>(r_0 + 1024); res[3] = lds_rd<u32x2>(r_1 + 1024);
        }
        if constexpr (h == 9) lds_wr<f32x2>(p_w, f32x2{t_sum[0], diag(t_sq)});
        if constexpr (h == 11) {
          f32x4 p0 = lds_rd<f32x4>(p_r), p1 = lds_rd<f32x4>(p_r + 16);
          const float sq = diag(s_sq);
          const f32x2 pp = lo2(p0) + hi2(p0) + lo2(p1) + hi2(p1);
          const float a1 = dg ? s_sum[0] : pp[0], a2 = dg ? sq : pp[1];
          const float mu = a1 * (1.f / 512.f);
          const float rs = __builtin_amdgcn_rsqf(fmaxf(a2 * (1.f / 512.f) - mu * mu, 0.f) + 1e-5f);
          lds_wr<float>(s_w, rs); lds_wr<float>(s_w + 320, -mu);
        }
        if constexpr (h == 13) {
#pragma unroll
          for (int m = 0; m < 5; ++m) vnm[m] = lds_rd<f32x4>(st_l + m * 64);
        }
        if constexpr (h == 9) fcnt = lds_flag(L_FLG1 + FL_F);
        if constexpr (h == 11) asm volatile("buffer_load_dword %0, %1, %2, 0 offen sc1" : "=v"(fk) : "v"(fk_off), "s"(RS) : "memory");
        if constexpr (h == 12 || h == 13) sfor<(h - 12) * 4, (h - 12) * 4 + 4>(issueWo);
        if constexpr (h == 14) {
          asm volatile("s_waitcnt vmcnt(8)" : "+v"(fk) :: "memory");
          ALD(uv[0], a_lane, us, 0); ALD(uv[1], a_lane, us, 1024); ALD(uv[2], a_lane, us, 2048); ALD(uv[3], a_lane, us, 3072);
        }
        SCHED();
      };
      SCHED();
      sfor<0, 6>(issueW);
      __builtin_amdgcn_s_setprio(1);
      asm volatile("s_waitcnt vmcnt(%4)" : "+v"(hA[0]), "+v"(hA[1]), "+v"(hB[0]), "+v"(hB[1]) : "n"(12) : "memory");
      bar_lgkm<0>();
      SCHED();
      trV(IC<0>{});
#pragma unroll
      for (int m = 0; m < 5; ++m) af[0][m] = afr1(m, 0);
      trV(IC<1>{});
      SCHED();
#pragma unroll
      for (int i = 0; i < 2; ++i) {
        u32 B0 = hB[i].x, B1 = hB[i].y;
        if (i == 1 && hb * 128 + 64 + lane == 255) { B0 = 0; B1 = 0; }
        const u32 A0 = hA[i].x, A1 = hA[i].y, A2 = hA[i].z, A3 = hA[i].w;
        auto sw16 = [](u32 v) { return __builtin_amdgcn_alignbit(v, v, 16); };
        auto mix = [](u32 lo, u32 hi) { return (lo & 0xffffu) | (hi & 0xffff0000u); };
        ts[i] = u32x4{sw16(A3), sw16(A2), sw16(A1), sw16(A0)}; tm[i] = u32x4{mix(A3, A2), mix(A2, A1), mix(A1, A0), mix(A0, B1)};
        ts2[i] = u32x4{ts[i][1], ts[i][2], ts[i][3], sw16(B1)}; tm2[i] = u32x4{tm[i][1], tm[i][2], tm[i][3], mix(B1, B0)};
        asm volatile("" : "+v"(ts[i]), "+v"(tm[i]), "+v"(ts2[i]), "+v"(tm2[i]));
      }
      SCHED();
      half(IC<0>{});
      asm volatile("s_waitcnt vmcnt(6) lgkmcnt(5)\n\ts_barrier" ::: "memory");
      SCHED();
      half(IC<1>{});
      sfor<1, 8>([&](auto DD) INL {
        constexpr int d = decltype(DD)::v;
        if constexpr (d <= 5) vmwait<6>();
        if constexpr (d == 5) asm volatile("s_waitcnt lgkmcnt(0)" ::: "memory");
        if constexpr (d == 6) { asm volatile("s_waitcnt vmcnt(1) lgkmcnt(0)" ::: "memory"); }
        if (d < 7) bar_lgkm<5>();
        SCHED();
        half(IC<2 * d>{});
        if constexpr (d == 7) {
          bar_lgkm<0>();
          SCHED();
        }
        half(IC<2 * d + 1>{});
      });
    }
    const float csv = row0_bcast(av[0][0]);
    u32x2 Yv[4];
    auto v_out = [&](u32 l_v, u32 l_fl) INL {
      const u32 v_l = opaque(l_v + hh * 2560 + lane * 8);
      f32x2 a[10];
#pragma unroll
      for (int m = 0; m < 5; ++m) { a[2 * m] = pkfma(lo2(vnm[m]), bc2(csv), lo2(av[m])); a[2 * m + 1] = pkfma(hi2(vnm[m]), bc2(csv), hi2(av[m])); }
      SCHED();
#pragma unroll
      for (int m = 0; m < 5; ++m) lds_wr<u32x2>(v_l + m * 512, u32x2{pk2v(a[2 * m]), pk2v(a[2 * m + 1])});
      lds_signal(l_fl + FL_V + 4 * hh);
    };
    u32x4 w0, w1; u32 oo = 0;
    const rsrc_t RO = __builtin_amdgcn_make_buffer_rsrc((void*)out, 0, 2048 * 512 * 2, 0x00020000);
    auto outproj = [&](auto LAYER) INL {
      constexpr int layer = decltype(LAYER)::v, PFL = layer ? PF2 : PF;
      const u32 l_fl = layer ? L_FLG2 : L_FLG1;
      u32 wa[4];
#pragma unroll
      for (int nt = 0; nt < 4; ++nt) wa[nt] = opaque(L_WO + q * 16384 + tr_row + ((nt ^ tr_sw) * 32u));
      bf16x8 wf[64];
      if (layer == 0) {
        lds_signal(L_FLG1 + FL_WO);
        lds_wait(L_FLG1 + FL_WO, 4);
      }
#pragma unroll
      for (int i = 0; i < PFL; ++i) wf[i] = tr_frag(wa[i & 3] + (i >> 2) * 1024);
      f32x4 ac[4];
#pragma unroll
      for (int nt = 0; nt < 4; ++nt) ac[nt] = f32x4{bflo(res[nt].x), bfhi(res[nt].x), bflo(res[nt].y), bfhi(res[nt].y)};
      const u32 ob = (layer ? WS_O2B : WS_O1B) + (t * 4 + wb) * 16384;
      const u32 stb = (layer ? WS_ST2 : WS_ST1) + (t * 4 + wb) * 1024;
      lds_wait(l_fl + FL_O, 4);
      __builtin_amdgcn_s_setprio(layer ? 2 : 1);
      u32x4 ov[16];
#define OV4(M, G) M(ov[4 * G], a_lane, ob + G * 4096, 0); M(ov[4 * G + 1], a_lane, ob + G * 4096, 1024); M(ov[4 * G + 2], a_lane, ob + G * 4096, 2048); M(ov[4 * G + 3], a_lane, ob + G * 4096, 3072)
      OV4(ALD, 0); __builtin_amdgcn_s_sleep(OPACE); OV4(ALD, 1); __builtin_amdgcn_s_sleep(OPACE);
      OV4(ALD, 2); __builtin_amdgcn_s_sleep(OPACE); OV4(ALD, 3);
      sfor<0, 4>([&](auto GR) INL {
        constexpr int gr = decltype(GR)::v;
        asm volatile("s_waitcnt vmcnt(%4)" : "+v"(ov[4 * gr]), "+v"(ov[4 * gr + 1]), "+v"(ov[4 * gr + 2]), "+v"(ov[4 * gr + 3]) : "n"(12 - 4 * gr) : "memory");
#define OVW(G, N) asm volatile("s_waitcnt vmcnt(%4)" : "+v"(ov[4 * G]), "+v"(ov[4 * G + 1]), "+v"(ov[4 * G + 2]), "+v"(ov[4 * G + 3]) : "n"(N) : "memory")
#define OVBAD(G) (!all_ne(mx4(ov[4 * G + 3], mx4(ov[4 * G + 2], mx4(ov[4 * G + 1], mx4(ov[4 * G], 0)))), SENT))
#pragma nounroll
        for (u32 sp = 0; sp < SPIN_CAP; ++sp) {
          if (!OVBAD(gr)) break;
          OV4(RLD, gr);
          if constexpr (gr < 1) { OVW(1, 12); if (OVBAD(1)) { OV4(RLD, 1); } }
          if constexpr (gr < 2) { OVW(2, 8); if (OVBAD(2)) { OV4(RLD, 2); } }
          if constexpr (gr < 3) { OVW(3, 4); if (OVBAD(3)) { OV4(RLD, 3); } }
          OVW(gr, 0);
        }
        SCHED();
#pragma unroll
        for (int i = 16 * gr; i < 16 * gr + 16; ++i) {
          ac[i & 3] = mfma32(wf[i], __builtin_bit_cast(bf16x8, ov[i >> 2]), ac[i & 3]);
          if (i + PFL < 64) wf[i + PFL] = tr_frag(wa[(i + PFL) & 3] + ((i + PFL) >> 2) * 1024);
        }
#pragma unroll
        for (int i = 0; i < 16; ++i) { if (16 * gr + i + PFL < 64) { SGB(0x8, 1); SGB(0x100, 2); } }
        SCHED();
      });
      f32x2 p1 = lo2(ac[0]) + hi2(ac[0]), p2 = lo2(ac[0]) * lo2(ac[0]);
      p2 = pkfma(hi2(ac[0]), hi2(ac[0]), p2);
#pragma unroll
      for (int nt = 1; nt < 4; ++nt) {
        p1 += lo2(ac[nt]); p2 = pkfma(lo2(ac[nt]), lo2(ac[nt]), p2);
        p1 += hi2(ac[nt]); p2 = pkfma(hi2(ac[nt]), hi2(ac[nt]), p2);
      }
      float s1 = p1[0] + p1[1], s2 = p2[0] + p2[1];
      swap16(s1, s2); s1 += s2; s2 = s1;
      swap32(s1, s2); s1 += s2;
      if (layer == 0) vmwait<0>();
      if (lane < 32) __builtin_amdgcn_raw_buffer_store_b32(min(__float_as_uint(s1), SENT - 1), R, lane * 4, stb + g * 128, 16);
      f32x2 yf[8];
      u32 ga[5], gb[5];
      const u32 go = opaque(q * 256 + c * 4), gh = opaque((lane & 7) * 128 + WS_ST1 + (tp * 4 + 3) * 1024);
      constexpr int NG = layer ? 4 : 5;
      auto gld = [&](u32 (&v)[5]) INL {
        asm volatile("buffer_load_dword %0, %4, %5, %6 offen sc1\n\tbuffer_load_dword %1, %4, %5, %6 offen offset:64 sc1\n\t"
                     "buffer_load_dword %2, %4, %5, %6 offen offset:128 sc1\n\tbuffer_load_dword %3, %4, %5, %6 offen offset:192 sc1"
                     : "+v"(v[0]), "+v"(v[1]), "+v"(v[2]), "+v"(v[3]) : "v"(go), "s"(RS), "s"(stb) : "memory");
        if (layer == 0) asm volatile("buffer_load_dword %0, %1, %2, 0 offen sc1" : "+v"(v[4]) : "v"(gh), "s"(RS) : "memory");
      };
      auto gok = [&](u32 (&v)[5]) INL {
        asm volatile("s_waitcnt vmcnt(%5)" : "+v"(v[0]), "+v"(v[1]), "+v"(v[2]), "+v"(v[3]), "+v"(v[4]) : "n"(NG) : "memory");
        return all_ne(mx3(mx3(v[0], v[1], v[4]), v[2], v[3]), SENT);
      };
#pragma unroll
      for (int i = 0; i < 5; ++i) { ga[i] = 0; gb[i] = 0; }
      if (layer) {
#pragma unroll
      for (int nt = 0; nt < 4; ++nt) { yf[2 * nt] = f32x2{bflo(Yv[nt].x), bfhi(Yv[nt].x)}; yf[2 * nt + 1] = f32x2{bflo(Yv[nt].y), bfhi(Yv[nt].y)}; }
      }
      SCHED();
      __builtin_amdgcn_s_sleep(layer ? DLY_S : DLY_S1);
      gld(ga);
      __builtin_amdgcn_s_sleep(DLY_S2);
      gld(gb);
      bool useb = false;
#pragma nounroll
      for (u32 sp = 0; sp < SPIN_CAP; ++sp) {
        if (gok(ga)) break;
        gld(ga);
        if (gok(gb)) { useb = true; break; }
        gld(gb);
      }
      u32 g0, g1, g2, g3;
      { const u32 a0 = ga[0], a1 = ga[1], a2 = ga[2], a3 = ga[3], b0 = gb[0], b1 = gb[1], b2 = gb[2], b3 = gb[3];
        const unsigned long long msk = useb ? ~0ull : 0ull;
        asm volatile("v_cndmask_b32 %0, %4, %8, %12\n\tv_cndmask_b32 %1, %5, %9, %12\n\tv_cndmask_b32 %2, %6, %10, %12\n\tv_cndmask_b32 %3, %7, %11, %12"
                     : "=&v"(g0), "=&v"(g1), "=&v"(g2), "=&v"(g3)
                     : "v"(a0), "v"(a1), "v"(a2), "v"(a3), "v"(b0), "v"(b1), "v"(b2), "v"(b3), "s"(msk)); }
      s1 = __uint_as_float(g0) + __uint_as_float(g2); s2 = __uint_as_float(g1) + __uint_as_float(g3);
      swap16(s1, s2); s1 += s2; s2 = s1;
      swap32(s1, s2); s1 += s2; s2 = s1;
      swap16(s1, s2);
      const float mu = s1 * (1.f / 512.f);
      const float rs = __builtin_amdgcn_rsqf(fmaxf(s2 * (1.f / 512.f) - mu * mu, 0.f) + 1e-5f);
      const float nmr = -mu * rs;
      f32x2 n[8];
#pragma unroll
      for (int nt = 0; nt < 4; ++nt) { n[2 * nt] = pkfma(lo2(ac[nt]), bc2(rs), bc2(nmr)); n[2 * nt + 1] = pkfma(hi2(ac[nt]), bc2(rs), bc2(nmr)); }
      SCHED();
      if (layer == 0) lds_signal(L_FLG2 + FL_ST);
      if (layer == 0) {
        lds_wait(L_FLG2 + FL_Y, 4);
        const u32 y_l = opaque(L_V2 + (4 * q + ((lane & 15) >> 2)) * 160 + wb * 32 + (lane & 3) * 8);
#pragma unroll
        for (int nt = 0; nt < 4; ++nt) Yv[nt] = lds_tr(y_l + nt * 2560);
      }
      if (layer == 0) {
#pragma unroll
      for (int nt = 0; nt < 4; ++nt) { yf[2 * nt] = f32x2{bflo(Yv[nt].x), bfhi(Yv[nt].x)}; yf[2 * nt + 1] = f32x2{bflo(Yv[nt].y), bfhi(Yv[nt].y)}; }
      }
      SCHED();
#pragma unroll
      for (int nt = 0; nt < 4; ++nt) { n[2 * nt] = pkfma(n[2 * nt], yf[2 * nt], lo2(ac[nt])); n[2 * nt + 1] = pkfma(n[2 * nt + 1], yf[2 * nt + 1], hi2(ac[nt])); }
      SCHED();
#pragma unroll
      for (int nt = 0; nt < 4; ++nt) { res[nt].x = pk2v(n[2 * nt]); res[nt].y = pk2v(n[2 * nt + 1]); }
      w0 = widen(res[0], res[1]); w1 = widen(res[2], res[3]);
      if (layer == 0) { const u32 xl = ((2 * (q & 1) + (q >> 1)) * 16 + c) * 16; st16(R, xl, sane(w0), xo); st16(R, xl + 1024, sane(w1), xo); }
      else { st16(RO, oo, w0); st16(RO, oo + 64, w1); }
      asm volatile("s_waitcnt vmcnt(2)" : "+v"(ga[0]), "+v"(ga[1]), "+v"(ga[2]), "+v"(ga[3]), "+v"(ga[4]),
                                         "+v"(gb[0]), "+v"(gb[1]), "+v"(gb[2]), "+v"(gb[3]), "+v"(gb[4]) :: "memory");
    };

    if (wb == 0 && lane < 16) lds_wr<u32>(L_FLG2 + lane * 4, 0u);
    v_out(L_V1, L_FLG1);
    if (__builtin_expect(!all_ne(fk, 0) || __builtin_amdgcn_readfirstlane(fcnt) < 1, 0)) {
      lds_wait(L_FLG1 + FL_F, 1); wait_flags(R, fk_off, 0);
      RLD(uv[0], a_lane, us, 0); RLD(uv[1], a_lane, us, 1024); RLD(uv[2], a_lane, us, 2048); RLD(uv[3], a_lane, us, 3072);
    }
    __builtin_amdgcn_s_setprio(0);
    sfor<8, 16>(issueWo);
    asm volatile("buffer_load_dword %0, %1, %2, %3 offen sc1" : "=v"(fv) : "v"(f_off), "s"(RS), "s"(WS_FC) : "memory");
#pragma unroll
    for (int i = 0; i < 2; ++i) {
      lds_wr<u32x4>(t_l + i * 1024 + 0 * 4160, u32x4{ts[i][0], ts[i][1], ts[i][2], ts[i][3]});
      lds_wr<u32x4>(t_l + i * 1024 + 1 * 4160, u32x4{tm[i][0], tm[i][1], tm[i][2], tm[i][3]});
      lds_wr<u32x4>(t_l + i * 1024 + 2 * 4160, ts2[i]);
      lds_wr<u32x4>(t_l + i * 1024 + 3 * 4160, tm2[i]);
    }
    if (lane < 8) lds_wr<u32x4>(tb0 + (lane >> 1) * 4160 + 4096 + (lane & 1) * 16, u32x4{0, 0, 0, 0});
    {
      const u32 ub = L_U + hh * 4576, u_l = opaque(ub + 480 + a_lane);
      if (lane < 30) lds_wr<u32x4>(ub + lane * 16, u32x4{0, 0, 0, 0});
      asm volatile("s_waitcnt vmcnt(%4)" : "+v"(uv[0]), "+v"(uv[1]), "+v"(uv[2]), "+v"(uv[3]) : "n"(9) : "memory");
#pragma unroll
      for (int i = 0; i < 4; ++i) lds_wr<u32x4>(u_l + i * 1024, uv[i]);
      lds_signal(L_FLG1 + FL_CV + (hh >> 1) * 4);
    }
    st16(R, a_lane, ones4, xo); st16(R, a_lane + 1024, ones4, xo);
    st8(R, c * 8, u32x2{SENT, SENT}, WS_X2S + ((t * 4 + wb) * 8 + g) * 128);
    asm volatile("s_waitcnt vmcnt(3)" : "+v"(fv) :: "memory");
    if (__builtin_expect(!all_ne(fv, 0), 0)) wait_flags(R, f_off, WS_FC);
    outproj(IC<0>{});
    {
      float s1 = 0.f, s2 = 0.f, s3 = 0.f, s4 = 0.f;
#pragma unroll
      for (int nt = 0; nt < 4; ++nt) {
        s1 = dot2(res[nt].x, ONE2, s1); s2 = dot2(res[nt].x, res[nt].x, s2);
        s3 = dot2(res[nt].y, ONE2, s3); s4 = dot2(res[nt].y, res[nt].y, s4);
      }
      s1 = xq_add(s1 + s3); s2 = xq_add(s2 + s4);
      if (lane < 16) st8(R, c * 8, u32x2{min(__float_as_uint(s1), SENT - 1), min(__float_as_uint(s2), SENT - 1)}, WS_X2S + ((t * 4 + wb) * 8 + g) * 128);
    }
    u32x4 pa_, pb_;
    const u32 ps_a = WS_X2S + (t * 4 + wb) * 1024, ps_b = WS_X2S + (tp * 4 + 3) * 1024;
    lds_wait(L_FLG2 + FL_ST, 4);
    u32x4 xv0[5], xv1[5];
    __builtin_amdgcn_s_sleep(DLY_XB);
    x2_load(xv0, H1);
    __builtin_amdgcn_s_setprio(1);
    X2_WAIT(xv0, 0);
    x2_put(xv0, H1);
    bar_lgkm<0>();
#pragma unroll
    for (int i = 0; i < 5; ++i) av[i] = zero4;
    const u32 xb0 = opaque(L_B + a_lane), xb4 = opaque(L_B + 65536 + a_lane);
    {
      bf16x8 af[2][5];
      auto afr2 = [&](int m, int ks) INL { return m < 4 ? lds_rd<bf16x8>(xb0 + (m * 16 + ks) * 1024) : lds_rd<bf16x8>(xb4 + ks * 1024); };
#pragma unroll
      for (int m = 0; m < 5; ++m) af[0][m] = afr2(m, H1 * 8);
      SCHED();
      sfor<0, 16>([&](auto H) INL {
        constexpr int h = decltype(H)::v;
        constexpr int ks = h ^ (H1 * 8), kn = (h + 1) ^ (H1 * 8);
        if constexpr (h == 0) { x2_load(xv1, 1 - H1); x2_load(xv1, 1 - H1, -4); ALD(pa_, a_lane, ps_a, 0); ALD(pb_, a_lane, ps_b, 0); SCHED(); }
        if constexpr (h == 7) { X2_WAIT(xv1, 2); x2_put(xv1, 1 - H1); x2_put(xv1, 1 - H1, -4); SCHED(); }
        if constexpr (h == 7) {
          asm volatile("s_waitcnt vmcnt(0)" : "+v"(pa_), "+v"(pb_) :: "memory");
#pragma nounroll
          for (u32 sp = 0; sp < SPIN_CAP; ++sp) {
            if (all_ne(mx4(pb_, mx4(pa_, 0)), SENT)) break;
            RLD(pa_, a_lane, ps_a, 0); RLD(pb_, a_lane, ps_b, 0);
            asm volatile("s_waitcnt vmcnt(0)" : "+v"(pa_), "+v"(pb_) :: "memory");
          }
    {
      float v[8] = {__uint_as_float(pa_.x), __uint_as_float(pa_.y), __uint_as_float(pa_.z), __uint_as_float(pa_.w),
                    __uint_as_float(pb_.x), __uint_as_float(pb_.y), __uint_as_float(pb_.z), __uint_as_float(pb_.w)};
#pragma unroll
      for (int i = 0; i < 4; ++i) { swap32(v[i], v[i + 4]); v[i] += v[i + 4]; }
#pragma unroll
      for (int i = 0; i < 2; ++i) { swap16(v[i], v[i + 2]); v[i] += v[i + 2]; }
      const float a1 = ror8_add(v[0]), a2 = ror8_add(v[1]);
      const float mu = a1 * (1.f / 512.f);
      const float rs = __builtin_amdgcn_rsqf(fmaxf(a2 * (1.f / 512.f) - mu * mu, 0.f) + 1e-5f);
      const u32 sa = L_ST2 + ((q >> 1 ? 0 : wb + 1) * 16 + 2 * (lane & 7) + (q & 1)) * 4;
      lds_wr<float>(sa, rs); lds_wr<float>(sa + 320, -mu);
    }
          bar_lgkm<0>(); SCHED();
        }
        if constexpr (h == 14) {
          const u32 st_l = opaque(L_ST2 + 320 + q * 16);
#pragma unroll
          for (int m = 0; m < 5; ++m) vnm[m] = lds_rd<f32x4>(st_l + m * 64);
          SCHED();
        }
        sfor<0, 5>([&](auto I) INL {
          constexpr int i = decltype(I)::v;
          av[i] = mfma32(af[h & 1][i], Wv[ks], av[i]);
          if constexpr (h < 15 && 2 * i < 5) af[(h + 1) & 1][2 * i] = afr2(2 * i, kn);
          if constexpr (h < 15 && 2 * i + 1 < 5) af[(h + 1) & 1][2 * i + 1] = afr2(2 * i + 1, kn);
          SCHED();
        });
      });
    }
    v_out(L_V2, L_FLG2);
    __builtin_amdgcn_s_setprio(0);
    oo = opaque((u32)(s0 + 16 * wb + c) * 1024u + g * 128u + 32 * (q & 1) + 16 * (q >> 1));
    outproj(IC<1>{});
  }
}

extern "C" void kernel_launch(void* const* d_in, const int* in_sizes, int n_in, void* d_out, int out_size,
                              void* d_ws, size_t ws_size, hipStream_t stream) {
  (void)hipFuncSetAttribute((const void*)fused_kernel, hipFuncAttributeMaxDynamicSharedMemorySize, LDS_BYTES);
  fused_kernel<<<256, 512, LDS_BYTES, stream>>>((const u16*)d_in[0], (const u16*)d_in[1], (const u16*)d_in[2],
                                                (const u16*)d_in[4], (const u16*)d_in[5], (const u16*)d_in[6],
                                                (const u16*)d_in[7], (const u16*)d_in[8], (u16*)d_out, (char*)d_ws);
}
```
